```python
import torch

def _softmax(x, axis=-1):
    return torch.softmax(x, dim=axis)
def _gelu(x):
    return torch.nn.functional.gelu(x, approximate="tanh")
def _silu(x):
    return torch.nn.functional.silu(x)
def _sigmoid(x):
    return torch.sigmoid(x)
def _layernorm(x, axis=-1, eps=1e-5):
    m = x.mean(dim=axis, keepdim=True)
    v = x.var(dim=axis, keepdim=True, unbiased=False)
    return (x - m) / torch.sqrt(v + eps)
def _scatter_add(tgt, idx, src, axis=0):
    return tgt.clone().index_add_(axis, idx, src)
def _segment_sum(src, seg, n):
    out = torch.zeros((n,) + src.shape[1:], dtype=src.dtype)
    return out.index_add_(0, seg, src)
def _pad(x, pw):
    flat = tuple(v for pair in reversed(pw) for v in pair)
    return torch.nn.functional.pad(x, flat)

kernel_name = 'interleave_layers__spectral_fnet__spectral_fnet__prompt'

def setup_inputs():

    _w = (lambda a: (lambda t: t.to(torch.bfloat16) if t.is_floating_point() else t)(torch.as_tensor(a)))
    def _A_setup():
        B, S, D = 1, 2048, 640
        ns = torch.arange(S)[:, None] * torch.arange(S)[None, :]
        nd = torch.arange(D)[:, None] * torch.arange(D)[None, :]
        Ws_re = torch.cos(-2.0 * torch.pi * ns / S).to(float)
        Ws_im = torch.sin(-2.0 * torch.pi * ns / S).to(float)
        Wd_re = torch.cos(-2.0 * torch.pi * nd / D).to(float)
        Wd_im = torch.sin(-2.0 * torch.pi * nd / D).to(float)
        return {
            "x": _w((lambda *s: torch.randn(*s, dtype=torch.bfloat16) * 0.05)(B, S, D)),
            "dft_seq_re": _w(Ws_re),
            "dft_seq_im": _w(Ws_im),
            "dft_hid_re": _w(Wd_re),
            "dft_hid_im": _w(Wd_im),
        }
    _A_in = _A_setup()

    def _B_setup():
        B, S, D = 1, 2048, 640
        ns = torch.arange(S)[:, None] * torch.arange(S)[None, :]
        nd = torch.arange(D)[:, None] * torch.arange(D)[None, :]
        Ws_re = torch.cos(-2.0 * torch.pi * ns / S).to(float)
        Ws_im = torch.sin(-2.0 * torch.pi * ns / S).to(float)
        Wd_re = torch.cos(-2.0 * torch.pi * nd / D).to(float)
        Wd_im = torch.sin(-2.0 * torch.pi * nd / D).to(float)
        return {
            "x": _w((lambda *s: torch.randn(*s, dtype=torch.bfloat16) * 0.05)(B, S, D)),
            "dft_seq_re": _w(Ws_re),
            "dft_seq_im": _w(Ws_im),
            "dft_hid_re": _w(Wd_re),
            "dft_hid_im": _w(Wd_im),
        }
    _B_in = _B_setup()

    return {
        "A_x": _A_in["x"],
        "A_dft_seq_re": _A_in["dft_seq_re"],
        "A_dft_seq_im": _A_in["dft_seq_im"],
        "A_dft_hid_re": _A_in["dft_hid_re"],
        "A_dft_hid_im": _A_in["dft_hid_im"],
        "B_x": _B_in["x"],
        "B_dft_seq_re": _B_in["dft_seq_re"],
        "B_dft_seq_im": _B_in["dft_seq_im"],
        "B_dft_hid_re": _B_in["dft_hid_re"],
        "B_dft_hid_im": _B_in["dft_hid_im"],
    }

def reference(A_x, A_dft_seq_re, A_dft_seq_im, A_dft_hid_re, A_dft_hid_im, B_x, B_dft_seq_re, B_dft_seq_im, B_dft_hid_re, B_dft_hid_im):
    def _A_ref(x, dft_seq_re, dft_seq_im, dft_hid_re, dft_hid_im):
        a = torch.einsum("bsd,ed->bse", x, dft_hid_re)
        b = torch.einsum("bsd,ed->bse", x, dft_hid_im)
        y = torch.einsum("ts,bse->bte", dft_seq_re, a) - torch.einsum("ts,bse->bte", dft_seq_im, b)
        return y

    def _B_ref(x, dft_seq_re, dft_seq_im, dft_hid_re, dft_hid_im):
        a = torch.einsum("bsd,ed->bse", x, dft_hid_re)
        b = torch.einsum("bsd,ed->bse", x, dft_hid_im)
        y = torch.einsum("ts,bse->bte", dft_seq_re, a) - torch.einsum("ts,bse->bte", dft_seq_im, b)
        return y

    x = A_x
    x = x + _A_ref(_layernorm(x, axis=-1), A_dft_seq_re, A_dft_seq_im, A_dft_hid_re, A_dft_hid_im)
    x = x + _B_ref(_layernorm(x, axis=-1), B_dft_seq_re, B_dft_seq_im, B_dft_hid_re, B_dft_hid_im)
    x = x + _A_ref(_layernorm(x, axis=-1), A_dft_seq_re, A_dft_seq_im, A_dft_hid_re, A_dft_hid_im)
    x = x + _B_ref(_layernorm(x, axis=-1), B_dft_seq_re, B_dft_seq_im, B_dft_hid_re, B_dft_hid_im)
    return x
```

```cpp
#include <hip/hip_runtime.h>
#include <hip/hip_bf16.h>

typedef unsigned short u16;
typedef short bf16x8 __attribute__((ext_vector_type(8)));
typedef float f32x4 __attribute__((ext_vector_type(4)));

constexpr int S = 2048, D = 640;

__device__ __forceinline__ float bf2f(u16 v) { return __uint_as_float(((unsigned)v) << 16); }
__device__ __forceinline__ u16 f2bf(float f) { __hip_bfloat16 b = __float2bfloat16(f); return *(u16*)&b; }

__global__ void ln_kernel(const u16* __restrict__ x, u16* __restrict__ xn) {
    int row = blockIdx.x * 4 + (threadIdx.x >> 6), lane = threadIdx.x & 63;
    float v[10]; float s = 0.f;
    for (int i = 0; i < 10; ++i) { v[i] = bf2f(x[row * D + lane + 64 * i]); s += v[i]; }
    for (int o = 32; o >= 1; o >>= 1) s += __shfl_xor(s, o);
    float m = s * (1.0f / D); float q = 0.f;
    for (int i = 0; i < 10; ++i) { float d = v[i] - m; q += d * d; }
    for (int o = 32; o >= 1; o >>= 1) q += __shfl_xor(q, o);
    float r = 1.0f / sqrtf(q * (1.0f / D) + 1e-5f);
    for (int i = 0; i < 10; ++i) xn[row * D + lane + 64 * i] = f2bf((v[i] - m) * r);
}

template <int MODE>
__global__ void gemm_nt(const u16* __restrict__ A, const u16* __restrict__ B, const u16* __restrict__ A2, const u16* __restrict__ B2,
                        const u16* __restrict__ res, u16* __restrict__ C, int lda, int ldb, int ldc, int K, float sign) {
    __shared__ u16 sA[64][40], sB[64][40];
    int tid = threadIdx.x, wid = tid >> 6, lane = tid & 63;
    int wr = wid >> 1, wc = wid & 1, fr = lane & 15, fq = lane >> 4;
    int m0 = blockIdx.y * 64, n0 = blockIdx.x * 64;
    f32x4 acc[2][2] = {};
    for (int pass = 0; pass < (MODE == 1 ? 2 : 1); ++pass) {
        const u16* a = pass ? A2 : A; const u16* b = pass ? B2 : B;
        for (int k0 = 0; k0 < K; k0 += 32) {
            int r = tid >> 2, c = (tid & 3) * 8;
            *(bf16x8*)&sA[r][c] = *(const bf16x8*)&a[(size_t)(m0 + r) * lda + k0 + c];
            *(bf16x8*)&sB[r][c] = *(const bf16x8*)&b[(size_t)(n0 + r) * ldb + k0 + c];
            __syncthreads();
            bf16x8 af[2], bfr[2];
            for (int i = 0; i < 2; ++i) af[i] = *(const bf16x8*)&sA[wr * 32 + i * 16 + fr][fq * 8];
            for (int i = 0; i < 2; ++i) bfr[i] = *(const bf16x8*)&sB[wc * 32 + i * 16 + fr][fq * 8];
            for (int i = 0; i < 2; ++i) for (int j = 0; j < 2; ++j)
                acc[i][j] = __builtin_amdgcn_mfma_f32_16x16x32_bf16(af[i], bfr[j], acc[i][j], 0, 0, 0);
            __syncthreads();
        }
    }
    for (int i = 0; i < 2; ++i) for (int j = 0; j < 2; ++j) for (int q = 0; q < 4; ++q) {
        int m = m0 + wr * 32 + i * 16 + fq * 4 + q, n = n0 + wc * 32 + j * 16 + fr;
        float v = acc[i][j][q] * sign;
        if (MODE == 1) v += bf2f(res[(size_t)m * ldc + n]);
        C[(size_t)m * ldc + n] = f2bf(v);
    }
}

extern "C" void kernel_launch(void* const* d_in, const int* in_sizes, int n_in,
                              void* d_out, int out_size, void* d_ws, size_t ws_size,
                              hipStream_t stream) {
    const u16* x0 = (const u16*)d_in[0];
    const u16* Ws_re[2] = {(const u16*)d_in[1], (const u16*)d_in[6]};
    const u16* Ws_im[2] = {(const u16*)d_in[2], (const u16*)d_in[7]};
    const u16* Wd_re[2] = {(const u16*)d_in[3], (const u16*)d_in[8]};
    const u16* Wd_im[2] = {(const u16*)d_in[4], (const u16*)d_in[9]};
    char* ws = (char*)d_ws;
    u16* xn = (u16*)ws;
    u16* aT = (u16*)(ws + (4 << 20));
    u16* bT = (u16*)(ws + (8 << 20));
    u16* xb[2] = {(u16*)(ws + (12 << 20)), (u16*)(ws + (16 << 20))};
    const u16* xin = x0;
    for (int l = 0; l < 4; ++l) {
        int s = l & 1;
        u16* xout = (l == 3) ? (u16*)d_out : xb[l & 1];
        ln_kernel<<<S / 4, 256, 0, stream>>>(xin, xn);
        gemm_nt<0><<<dim3(S / 64, D / 64), 256, 0, stream>>>(Wd_re[s], xn, nullptr, nullptr, nullptr, aT, D, D, S, D, 1.0f);
        gemm_nt<0><<<dim3(S / 64, D / 64), 256, 0, stream>>>(Wd_im[s], xn, nullptr, nullptr, nullptr, bT, D, D, S, D, -1.0f);
        gemm_nt<1><<<dim3(D / 64, S / 64), 256, 0, stream>>>(Ws_re[s], aT, Ws_im[s], bT, xin, xout, S, S, D, S, 1.0f);
        xin = xout;
    }
}
```

```cpp
#include <hip/hip_runtime.h>
#include <hip/hip_bf16.h>
#include <stdint.h>

typedef unsigned short u16;
typedef unsigned long long u64;
typedef short bf16x8 __attribute__((ext_vector_type(8)));
typedef float f32x4 __attribute__((ext_vector_type(4)));
typedef float f32x2 __attribute__((ext_vector_type(2)));
typedef unsigned u32x4 __attribute__((ext_vector_type(4)));
typedef unsigned u32x2 __attribute__((ext_vector_type(2)));
typedef __bf16 bf16x2v __attribute__((ext_vector_type(2)));
#define LAS __attribute__((address_space(3)))
#define GAS __attribute__((address_space(1)))

namespace {

constexpr int SEQ = 2048, DM = 640;

constexpr size_t WS_GEN = 0;
constexpr size_t WS_ERR = 256;
constexpr size_t WS_FR = 4096;
constexpr size_t WS_FH = WS_FR + 4 * 256 * 128;
constexpr size_t WS_FS = WS_FH + 4 * 512 * 128;
constexpr size_t XN_BYTES = (size_t)SEQ * DM * 2;
constexpr size_t WS_XN = 1u << 20;
constexpr size_t WS_AB = WS_XN + 4 * XN_BYTES;
constexpr size_t AB_BYTES = 2 * XN_BYTES;
constexpr size_t WS_P = WS_AB + 4 * AB_BYTES;
constexpr size_t P_BYTES = 4 * XN_BYTES;
constexpr size_t WS_END = WS_P + 4 * P_BYTES;

constexpr int L_MISC = 0;
constexpr int L_AB = 4096;
constexpr int L_C = 4096;
constexpr int L_WD = 98304;
constexpr int L_WA = 114688;
constexpr int LDS_BYTES = 163840;
constexpr int CNT_R = 0, CNT_H0 = 16, CNT_H1 = 32, CNT_S = 48, CNT_R0 = 64, STATS = 256;

#define WAIT_V(n) asm volatile("s_waitcnt vmcnt(%0)" ::"n"(n) : "memory")
#define WAIT_L(n) asm volatile("s_waitcnt lgkmcnt(%0)" ::"n"(n) : "memory")
#define BAR() do { asm volatile("" ::: "memory"); __builtin_amdgcn_s_barrier(); asm volatile("" ::: "memory"); } while (0)
#define SCHED() __builtin_amdgcn_sched_barrier(0)

__device__ __forceinline__ unsigned pk_bf16(float a, float b) {
    const f32x2 v = {a, b};
    return __builtin_bit_cast(unsigned, __builtin_convertvector(v, bf16x2v));
}
__device__ __forceinline__ float bf_lo(unsigned u) { return __builtin_bit_cast(float, u << 16); }
__device__ __forceinline__ float bf_hi(unsigned u) { return __builtin_bit_cast(float, u & 0xffff0000u); }

__device__ __forceinline__ int perm80(int r) {
    return r < 64 ? ((r >> 5) * 32 + ((r & 15) >> 2) * 8 + ((r >> 4) & 1) * 4 + (r & 3)) : r;
}

__device__ __forceinline__ float sum_mod8(float v) {
    v += __builtin_bit_cast(float, __builtin_amdgcn_update_dpp(0, __builtin_bit_cast(int, v), 0x128  , 0xf, 0xf, false));
    { const unsigned u = __builtin_bit_cast(unsigned, v); const unsigned u2 = u;
      const auto r = __builtin_amdgcn_permlane16_swap(u, u2, false, false);
      const unsigned r0 = r[0], r1 = r[1]; v = __builtin_bit_cast(float, r0) + __builtin_bit_cast(float, r1); }
    { const unsigned u = __builtin_bit_cast(unsigned, v); const unsigned u2 = u;
      const auto r = __builtin_amdgcn_permlane32_swap(u, u2, false, false);
      const unsigned r0 = r[0], r1 = r[1]; v = __builtin_bit_cast(float, r0) + __builtin_bit_cast(float, r1); }
    return v;
}

constexpr unsigned SPIN_CAP = 400000;

__device__ __forceinline__ void poll16(const char* p  , u64 tag, int lane, char* ws) {
    const GAS u64* f = (const GAS u64*)p;
    GAS unsigned* err = (GAS unsigned*)(ws + WS_ERR);
    for (unsigned it = 0;; ++it) {
        u64 v = tag;
        if (lane < 16) v = __hip_atomic_load(f, __ATOMIC_RELAXED, __HIP_MEMORY_SCOPE_AGENT);
        if (__all(v == tag)) break;
        if ((it & 255u) == 255u) {
            if (__builtin_amdgcn_readfirstlane(__hip_atomic_load(err, __ATOMIC_RELAXED, __HIP_MEMORY_SCOPE_AGENT)) == 0x600DBADu) break;
            if (it > SPIN_CAP) { if (lane == 0) __hip_atomic_store(err, 0x600DBADu, __ATOMIC_RELAXED, __HIP_MEMORY_SCOPE_AGENT); break; }
        }
        __builtin_amdgcn_s_sleep(1);
    }
}

__device__ __forceinline__ void signal_last(LAS unsigned* cnt, unsigned last, char* flag, u64 tag, int lane) {
    asm volatile("s_waitcnt vmcnt(0)" ::: "memory");
    unsigned old = 0;
    if (lane == 0) old = __hip_atomic_fetch_add(cnt, 1u, __ATOMIC_RELAXED, __HIP_MEMORY_SCOPE_WORKGROUP);
    old = __builtin_amdgcn_readfirstlane(old);
    if (old == last && lane == 0) __hip_atomic_store((GAS u64*)flag, tag, __ATOMIC_RELAXED, __HIP_MEMORY_SCOPE_AGENT);
}

#define ST16_SC1(rs, v, voff, soff) __builtin_amdgcn_raw_buffer_store_b128((v), (rs), (voff) + (soff), 0, 16)
#define ST8_SC1(rs, v, voff, soff) __builtin_amdgcn_raw_buffer_store_b64((v), (rs), (voff) + (soff), 0, 16)
#define LD16x4_SC1(d0, d1, d2, d3, rs, voff) asm volatile("s_nop 4\n\tbuffer_load_dwordx4 %0, %4, %5, 0 offen sc1\n\tbuffer_load_dwordx4 %1, %4, %5, 0 offen offset:1024 sc1\n\t" \
    "buffer_load_dwordx4 %2, %4, %5, 0 offen offset:2048 sc1\n\tbuffer_load_dwordx4 %3, %4, %5, 0 offen offset:3072 sc1" \
    : "=&v"(d0), "=&v"(d1), "=&v"(d2), "=&v"(d3) : "v"(voff), "s"(rs) : "memory")
#define LD16x2_SC1(d0, d1, rs, voff) asm volatile("s_nop 4\n\tbuffer_load_dwordx4 %0, %2, %3, 0 offen sc1\n\tbuffer_load_dwordx4 %1, %2, %3, 0 offen offset:1024 sc1" \
    : "=&v"(d0), "=&v"(d1) : "v"(voff), "s"(rs) : "memory")
#define WAIT_VR(n, r) asm volatile("s_waitcnt vmcnt(%1)" : "+v"(r) : "n"(n) : "memory")

#define STAMP(i) do { } while (0)
#define STAMP2(i) do { } while (0)
#define STAMP_ARG
#define STAMP_PASS

__device__ __forceinline__ void load_c(LAS unsigned char* smem, const u16* C, int lw, int lane) {
#if defined(__HIP_DEVICE_COMPILE__)
    const __amdgpu_buffer_rsrc_t rC = __builtin_amdgcn_make_buffer_rsrc((void*)C, 0, 80 * DM * 2, 0x00020000);
    int vo[3];
    #pragma unroll
    for (int q = 0; q < 3; ++q) {
        const int rg = q < 2 ? lw + 4 * q : 8 + (lw & 1);
        const int row = 8 * rg + (lane >> 3);
        vo[q] = perm80(row) * (DM * 2) + (((lane & 7) ^ ((row >> 1) & 7)) * 16);
    }
    #pragma unroll
    for (int ks = 0; ks < 10; ++ks) {
        __builtin_amdgcn_raw_ptr_buffer_load_lds(rC, (LAS void*)(smem + L_C + ks * 10240 + lw * 1024), 16, vo[0], ks * 128, 0, 0);
        __builtin_amdgcn_raw_ptr_buffer_load_lds(rC, (LAS void*)(smem + L_C + ks * 10240 + (lw + 4) * 1024), 16, vo[1], ks * 128, 0, 0);
        if ((ks & 1) == (lw >> 1))
            __builtin_amdgcn_raw_ptr_buffer_load_lds(rC, (LAS void*)(smem + L_C + ks * 10240 + (8 + (lw & 1)) * 1024), 16, vo[2], ks * 128, 0, 0);
    }
#endif
}
__device__ __forceinline__ void warm_w(const u16* W, int e2, int lw, int lane, unsigned& junk) {
#if defined(__HIP_DEVICE_COMPILE__)
    const __amdgpu_buffer_rsrc_t rW = __builtin_amdgcn_make_buffer_rsrc((void*)W, 0, 128 * SEQ * 2, 0x00020000);
    const int vo = (32 * e2 + 8 * lw + (lane >> 4)) * (SEQ * 2) + (lane & 15) * 128;
    asm volatile("s_nop 4\n\tbuffer_load_dword %0, %1, %2, 0 offen" : "+v"(junk) : "v"(vo), "s"(rW) : "memory");
    asm volatile("s_nop 4\n\tbuffer_load_dword %0, %1, %2, 0 offen" : "+v"(junk) : "v"(vo + 4 * SEQ * 2), "s"(rW) : "memory");
#endif
}

__device__ __forceinline__ void r_first(LAS unsigned char* smem, char* ws, const u16* x_in, u64 gen, int tbH, int j, int w, int lane) {
#if defined(__HIP_DEVICE_COMPILE__)
    float v[24];
    const bool third = (lane >> 5) == (w & 1);
    const int slot2 = 8 + (w >> 1);
    if (w < 4) {
        const u16* xp = x_in + (size_t)(tbH * 128 + j * 8 + (lane & 7)) * DM + (lane >> 3) * 8;
        u32x4 x[3];
        x[0] = *(const u32x4*)(xp + w * 64); x[1] = *(const u32x4*)(xp + (w + 4) * 64);
        x[2] = (u32x4){0, 0, 0, 0};
        if (third) x[2] = *(const u32x4*)(xp + slot2 * 64);
        #pragma unroll
        for (int c = 0; c < 3; ++c)
            #pragma unroll
            for (int q = 0; q < 4; ++q) { v[c * 8 + 2 * q] = bf_lo(x[c][q]); v[c * 8 + 2 * q + 1] = bf_hi(x[c][q]); }
        float s = 0.f;
        #pragma unroll
        for (int q = 0; q < 24; ++q) s += v[q];
        const float mean_w = sum_mod8(s) * (1.0f / 160.0f);
        float m2 = 0.f, m3 = 0.f;
        #pragma unroll
        for (int q = 0; q < 16; ++q) { const float d = v[q] - mean_w; m2 += d * d; }
        #pragma unroll
        for (int q = 16; q < 24; ++q) { const float d = v[q] - mean_w; m3 += d * d; }
        if (third) m2 += m3;
        const float m2_w = sum_mod8(m2);
        if (lane < 8) *(LAS f32x2*)(smem + L_MISC + STATS + (lane * 4 + w) * 8) = (f32x2){mean_w, m2_w};
    }
    WAIT_L(0);
    BAR();
    if (w < 4) {
        const LAS f32x4* sp = (const LAS f32x4*)(smem + L_MISC + STATS + (lane & 7) * 32);
        const f32x4 s0 = sp[0], s1 = sp[1];
        const float mean = ((s0[0] + s0[2]) + (s1[0] + s1[2])) * 0.25f;
        float m2 = (s0[1] + s0[3]) + (s1[1] + s1[3]);
        { const float d0 = s0[0] - mean, d1 = s0[2] - mean, d2 = s1[0] - mean, d3 = s1[2] - mean; m2 += 160.0f * ((d0 * d0 + d1 * d1) + (d2 * d2 + d3 * d3)); }
        const float rstd = 1.0f / sqrtf(m2 * (1.0f / DM) + 1e-5f);
        const __amdgpu_buffer_rsrc_t rX = __builtin_amdgcn_make_buffer_rsrc((void*)(ws + WS_XN + (size_t)tbH * 163840 + (j >> 1) * 20480), 0, 20480, 0x00020000);
        const int vo = (lane >> 5) * 1024 + ((lane >> 3) & 3) * 256 + ((j & 1) * 8 + (lane & 7)) * 16;
        #pragma unroll
        for (int c = 0; c < 3; ++c) {
            u32x4 o;
            #pragma unroll
            for (int q = 0; q < 4; ++q) o[q] = pk_bf16((v[c * 8 + 2 * q] - mean) * rstd, (v[c * 8 + 2 * q + 1] - mean) * rstd);
            if (c == 0) ST16_SC1(rX, o, vo, w * 2048);
            else if (c == 1) ST16_SC1(rX, o, vo, (w + 4) * 2048);
            else if (third) ST16_SC1(rX, o, vo, slot2 * 2048);
        }
        signal_last((LAS unsigned*)(smem + L_MISC + CNT_R0), 3u, ws + WS_FR + ((size_t)tbH * 16 + j) * 128, gen + 1, lane);
    }
#endif
}

__device__ __forceinline__ void r_phase(LAS unsigned char* smem, char* ws, u16* out, u64 gen, int l, int tbH, int j, int w, int lane, u32x4 (&xr)[2] STAMP_ARG) {
#if defined(__HIP_DEVICE_COMPILE__)
    if (w == 0) poll16(ws + WS_FS + ((size_t)(l * 16 + tbH) * 16 + (lane & 15)) * 128, gen + 9 + l, lane, ws);
    BAR();
    STAMP(8 + 12 * l + 9);
    float v[16];
    const bool second = (lane >> 4) == (w & 3);
    const int slot2 = 8 + (w >> 2);
    {
        const __amdgpu_buffer_rsrc_t rP = __builtin_amdgcn_make_buffer_rsrc((void*)(ws + WS_P + (size_t)l * P_BYTES + (size_t)(tbH * 16 + j) * 10240), 0, (int)(3 * XN_BYTES + 10240), 0x00020000);
        u32x4 p[4][2];
        const int vo = lane * 16 + w * 1024;
        const int vo2 = second ? lane * 16 + slot2 * 1024 : 0x7ffffff0;
        #pragma unroll
        for (int q = 0; q < 4; ++q)
            asm volatile("s_nop 4\n\tbuffer_load_dwordx4 %0, %2, %4, %5 offen sc1\n\tbuffer_load_dwordx4 %1, %3, %4, %5 offen sc1"
                         : "=&v"(p[q][0]), "=&v"(p[q][1]) : "v"(vo), "v"(vo2), "s"(rP), "s"(q * (int)XN_BYTES) : "memory");
        STAMP2(20);
        asm volatile("s_waitcnt vmcnt(0)" : "+v"(p[0][0]), "+v"(p[0][1]), "+v"(p[1][0]), "+v"(p[1][1]), "+v"(p[2][0]), "+v"(p[2][1]), "+v"(p[3][0]), "+v"(p[3][1]) :: "memory");
        STAMP2(21);
        #pragma unroll
        for (int c = 0; c < 2; ++c)
            #pragma unroll
            for (int q = 0; q < 4; ++q) {
                const float lo = bf_lo(xr[c][q]) + ((bf_lo(p[0][c][q]) + bf_lo(p[1][c][q])) + (bf_lo(p[2][c][q]) + bf_lo(p[3][c][q])));
                const float hi = bf_hi(xr[c][q]) + ((bf_hi(p[0][c][q]) + bf_hi(p[1][c][q])) + (bf_hi(p[2][c][q]) + bf_hi(p[3][c][q])));
                const unsigned u = pk_bf16(lo, hi);
                xr[c][q] = u; v[c * 8 + 2 * q] = bf_lo(u); v[c * 8 + 2 * q + 1] = bf_hi(u);
            }
    }
    if (l == 3) {
        u16* op = out + (size_t)(tbH * 128 + j * 8 + (lane & 7)) * DM + (lane >> 3) * 8;
        *(u32x4*)(op + w * 64) = xr[0];
        if (second) *(u32x4*)(op + slot2 * 64) = xr[1];
        return;
    }
    {
        float s = 0.f;
        #pragma unroll
        for (int q = 0; q < 16; ++q) s += v[q];
        const float mean_w = sum_mod8(s) * (1.0f / 80.0f);
        float m2 = 0.f, m3 = 0.f;
        #pragma unroll
        for (int q = 0; q < 8; ++q) { const float d = v[q] - mean_w; m2 += d * d; }
        #pragma unroll
        for (int q = 8; q < 16; ++q) { const float d = v[q] - mean_w; m3 += d * d; }
        if (second) m2 += m3;
        const float m2_w = sum_mod8(m2);
        if (lane < 8) *(LAS f32x2*)(smem + L_MISC + STATS + (lane * 8 + w) * 8) = (f32x2){mean_w, m2_w};
    }
    STAMP2(22);
    WAIT_L(0);
    BAR();
    STAMP2(23);
    {
        const int ln = l + 1;
        const LAS f32x4* sp = (const LAS f32x4*)(smem + L_MISC + STATS + (lane & 7) * 64);
        const f32x4 s0 = sp[0], s1 = sp[1], s2 = sp[2], s3 = sp[3];
        const float mean = (((s0[0] + s0[2]) + (s1[0] + s1[2])) + ((s2[0] + s2[2]) + (s3[0] + s3[2]))) * 0.125f;
        float m2 = ((s0[1] + s0[3]) + (s1[1] + s1[3])) + ((s2[1] + s2[3]) + (s3[1] + s3[3]));
        { const float d0 = s0[0] - mean, d1 = s0[2] - mean, d2 = s1[0] - mean, d3 = s1[2] - mean, d4 = s2[0] - mean, d5 = s2[2] - mean, d6 = s3[0] - mean, d7 = s3[2] - mean;
          m2 += 80.0f * (((d0 * d0 + d1 * d1) + (d2 * d2 + d3 * d3)) + ((d4 * d4 + d5 * d5) + (d6 * d6 + d7 * d7))); }
        const float rstd = 1.0f / sqrtf(m2 * (1.0f / DM) + 1e-5f);
        const __amdgpu_buffer_rsrc_t rX = __builtin_amdgcn_make_buffer_rsrc((void*)(ws + WS_XN + (size_t)ln * XN_BYTES + (size_t)tbH * 163840 + (j >> 1) * 20480), 0, 20480, 0x00020000);
        const int vo = (lane >> 5) * 1024 + ((lane >> 3) & 3) * 256 + ((j & 1) * 8 + (lane & 7)) * 16;
        #pragma unroll
        for (int c = 0; c < 2; ++c) {
            u32x4 o;
            #pragma unroll
            for (int q = 0; q < 4; ++q) o[q] = pk_bf16((v[c * 8 + 2 * q] - mean) * rstd, (v[c * 8 + 2 * q + 1] - mean) * rstd);
            if (c == 0) ST16_SC1(rX, o, vo, w * 2048);
            else if (second) ST16_SC1(rX, o, vo, slot2 * 2048);
        }
        STAMP2(24);
        signal_last((LAS unsigned*)(smem + L_MISC + CNT_R), (unsigned)l * 8 + 7, ws + WS_FR + ((size_t)(ln * 16 + tbH) * 16 + j) * 128, gen + 1 + ln, lane);
    }
#endif
}

}

__global__ __launch_bounds__(512, 2) void fnet_persistent(const u16* __restrict__ x_in,
        const u16* __restrict__ WreA, const u16* __restrict__ WimA, const u16* __restrict__ CreA, const u16* __restrict__ CimA,
        const u16* __restrict__ WreB, const u16* __restrict__ WimB, const u16* __restrict__ CreB, const u16* __restrict__ CimB,
        u16* __restrict__ out, char* __restrict__ ws) {
#if defined(__HIP_DEVICE_COMPILE__)
    extern __shared__ __attribute__((aligned(16))) unsigned char smem_[];
    LAS unsigned char* smem = (LAS unsigned char*)smem_;
    const int tid = threadIdx.x;
    int lane = tid & 63;
    const int w0 = __builtin_amdgcn_readfirstlane(tid >> 6);
    const int b = blockIdx.x;
#define GEOM(bb) const int xcd = (bb) & 7, idx = (bb) >> 3; \
    const int kq = xcd & 3, tbS = (xcd >> 2) * 8 + (idx >> 2), e2 = idx & 3; \
    const int tbH = (xcd >> 1) * 4 + (idx >> 3), j = (xcd & 1) * 8 + (idx & 7); \
    const int halfS = kq >> 1, halfH = j >> 3, eb = j & 7; (void)halfS; (void)halfH; (void)eb; (void)e2; (void)tbS;

    STAMP(0);
    u64 gen = __hip_atomic_load((const GAS u64*)(ws + WS_GEN), __ATOMIC_RELAXED, __HIP_MEMORY_SCOPE_AGENT);
    gen = ((u64)(unsigned)__builtin_amdgcn_readfirstlane((unsigned)(gen >> 32)) << 32) | (unsigned)__builtin_amdgcn_readfirstlane((unsigned)gen);
    if (tid < 64) *(LAS unsigned*)(smem + L_MISC + tid * 4) = 0;
    WAIT_L(0);
    BAR();

    unsigned junk = 0;
    u32x4 xr[2];
    xr[0] = (u32x4){0, 0, 0, 0}; xr[1] = xr[0];
    {
        GEOM(b)
        const int w = w0;
        if (w >= 4) {
            load_c(smem, (halfH ? CimA : CreA) + (size_t)eb * 80 * DM, w - 4, lane);
        }
        STAMP(1);
        r_first(smem, ws, x_in, gen, tbH, j, w, lane);
        {
            const u16* xp = x_in + (size_t)(tbH * 128 + j * 8 + (lane & 7)) * DM + (lane >> 3) * 8;
            xr[0] = *(const u32x4*)(xp + w * 64);
            if ((lane >> 4) == (w & 3)) xr[1] = *(const u32x4*)(xp + (8 + (w >> 2)) * 64);
        }
    }
    STAMP(2);

    for (int l = 0; l < 4; ++l) {
        const int set = l & 1;
        asm volatile("" : "+v"(lane));
        int bb = b; asm volatile("" : "+s"(bb));
        int w = w0; asm volatile("" : "+s"(w));
        GEOM(bb)
        if (w == 0) poll16(ws + WS_FR + ((size_t)(l * 16 + tbH) * 16 + (lane & 15)) * 128, gen + 1 + l, lane, ws);
        if (w >= 4) { WAIT_V(0); asm volatile("" :: "v"(junk)); }
        BAR();
        STAMP(8 + 12 * l + 0);
        {
            const int g = w ^ 4;
            if (w >= 4) __builtin_amdgcn_s_setprio(2);
            const __amdgpu_buffer_rsrc_t rX = __builtin_amdgcn_make_buffer_rsrc((void*)(ws + WS_XN + (size_t)l * XN_BYTES + (size_t)tbH * 163840 + g * 20480), 0, 20480, 0x00020000);
            u32x4 xf[20];
            const int vo = lane * 16;
            #pragma unroll
            for (int q = 0; q < 5; ++q) LD16x4_SC1(xf[4 * q], xf[4 * q + 1], xf[4 * q + 2], xf[4 * q + 3], rX, vo + q * 4096);
            STAMP2(1);
            const int fr = lane & 15, fq = lane >> 4;
            int rdC[2];
            #pragma unroll
            for (int kk = 0; kk < 2; ++kk) rdC[kk] = L_C + fr * 128 + (((4 * kk + fq) ^ ((fr >> 1) & 7)) * 16);
            f32x4 acc[5];
            #pragma unroll
            for (int i = 0; i < 5; ++i) acc[i] = (f32x4){0.f, 0.f, 0.f, 0.f};
            bf16x8 cf[2][5];
            #pragma unroll
            for (int i = 0; i < 5; ++i) cf[0][i] = *(const LAS bf16x8*)(smem + rdC[0] + i * 2048);
            #pragma unroll
            for (int ks = 0; ks < 20; ++ks) {
                if (ks + 1 < 20) {
                    #pragma unroll
                    for (int i = 0; i < 5; ++i) cf[(ks + 1) & 1][i] = *(const LAS bf16x8*)(smem + rdC[(ks + 1) & 1] + ((ks + 1) >> 1) * 10240 + i * 2048);
                }
                WAIT_VR(19 - ks, xf[ks]);
                if (ks == 0) STAMP2(2);
                if (ks == 10) STAMP2(3);
                if (ks == 19) STAMP2(4);
                #pragma unroll
                for (int i = 0; i < 5; ++i) acc[i] = __builtin_amdgcn_mfma_f32_16x16x32_bf16(__builtin_bit_cast(bf16x8, xf[ks]), cf[ks & 1][i], acc[i], 0, 0, 0);
            }
            STAMP2(5);
            const float sg = halfH ? -1.0f : 1.0f;
            unsigned d[5][2];
            #pragma unroll
            for (int i = 0; i < 5; ++i) { d[i][0] = pk_bf16(acc[i][0] * sg, acc[i][1] * sg); d[i][1] = pk_bf16(acc[i][2] * sg, acc[i][3] * sg); }
            const int sigma = 2 * tbH + (g >> 2);
            const __amdgpu_buffer_rsrc_t rA = __builtin_amdgcn_make_buffer_rsrc((void*)(ws + WS_AB + (size_t)l * AB_BYTES + ((size_t)(halfH * 4 + (eb >> 1)) * 32 + sigma) * 20480 + (eb & 1) * 10240 + (g & 3) * 2560), 0, 2560, 0x00020000);
            #pragma unroll
            for (int p = 0; p < 2; ++p) {
                const auto r0 = __builtin_amdgcn_permlane16_swap(d[2 * p][0], d[2 * p + 1][0], false, false);
                const auto r1 = __builtin_amdgcn_permlane16_swap(d[2 * p][1], d[2 * p + 1][1], false, false);
                const u32x4 o = {r0[0], r1[0], r0[1], r1[1]};
                const int so = (16 * (fq & 1) + fr) * 32 + (fq >> 1) * 16;
                ST16_SC1(rA, o, so, p * 1024);
            }
            { const u32x2 o = {d[4][0], d[4][1]}; ST8_SC1(rA, o, fr * 32 + fq * 8, 2048); }
            STAMP2(6);
            signal_last((LAS unsigned*)(smem + L_MISC + (w >= 4 ? CNT_H0 : CNT_H1)), (unsigned)l * 4 + 3,
                        ws + WS_FH + (((size_t)(l * 16 + tbH) * 16 + j) * 2 + (g >> 2)) * 128, gen + 5 + l, lane);
            if (w >= 4) __builtin_amdgcn_s_setprio(0);
        }
        STAMP(8 + 12 * l + 1);
        const char* hflag = ws + WS_FH + (((size_t)(l * 16 + 8 * (kq & 1) + ((lane & 15) >> 1)) * 16 + 8 * halfS + 2 * e2 + (lane & 1)) * 2) * 128;
        if (w >= 4) {
            const int lw = w - 4;
            const u16* W = set ? (halfS ? WimB : WreB) : (halfS ? WimA : WreA);
            const __amdgpu_buffer_rsrc_t rW = __builtin_amdgcn_make_buffer_rsrc((void*)(W + (size_t)tbS * 128 * SEQ + (kq & 1) * 1024), 0, 128 * SEQ * 2, 0x00020000);
            const __amdgpu_buffer_rsrc_t rA = __builtin_amdgcn_make_buffer_rsrc((void*)(ws + WS_AB + (size_t)l * AB_BYTES + ((size_t)(halfS * 4 + e2) * 32 + (kq & 1) * 16) * 20480), 0, 16 * 20480, 0x00020000);
            const int voffW = (lane >> 3) * (SEQ * 2) + (((lane & 7) ^ ((4 * (lw & 1) + (lane >> 4)) & 7)) * 16);
            const int voffA = lane * 16;
            auto sig = [&](int x) __attribute__((always_inline)) { return 2 * ((x + tbS) & 7) + (x >> 3); };
            auto issue_w = [&](int x) __attribute__((always_inline)) {
                const int sg_ = sig(x), dst = (x & 3) == 3 ? L_WD : L_WA + (x & 3) * 16384;
                #pragma unroll
                for (int q = 0; q < 4; ++q) { const int i = lw + 4 * q;
                    __builtin_amdgcn_raw_ptr_buffer_load_lds(rW, (LAS void*)(smem + dst + i * 1024), 16, voffW, sg_ * 128 + i * 8 * (SEQ * 2), 0, 0); }
            };
            auto issue_a = [&](int x) __attribute__((always_inline)) {
                const int sg_ = sig(x), dst = L_AB + (x & 3) * 20480;
                #pragma unroll
                for (int q = 0; q < 5; ++q) { const int i = lw + 4 * q;
                    __builtin_amdgcn_raw_ptr_buffer_load_lds(rA, (LAS void*)(smem + dst + i * 1024), 16, voffA, sg_ * 20480 + i * 1024, 0, 16); }
            };
            issue_w(0); issue_w(1); issue_w(2);
            if (w == 4) poll16(hflag, gen + 5 + l, lane, ws);
            BAR();
            STAMP(8 + 12 * l + 2);
            issue_a(0); issue_a(1);
            WAIT_V(5);
            BAR();
            STAMP(8 + 12 * l + 3);
            issue_a(2); issue_w(3);
            WAIT_V(9);  BAR(); issue_a(3);
            WAIT_V(9);  BAR(); issue_w(4); issue_a(4);
            for (int st = 2; st < 13; ++st) { WAIT_V(9); BAR(); issue_w(st + 3); issue_a(st + 3); }
            WAIT_V(9); BAR();
            WAIT_V(0); BAR();
            BAR();
            BAR();
            STAMP(8 + 12 * l + 4);
            STAMP2(13);
            if (l < 3) {
                volatile LAS unsigned* cs = (volatile LAS unsigned*)(smem + L_MISC + CNT_S);
                for (unsigned it = 0; it < 2000000u && (unsigned)__builtin_amdgcn_readfirstlane(*cs) < 4u * (unsigned)(l + 1); ++it) __builtin_amdgcn_s_sleep(2);
                load_c(smem, (set ? (halfH ? CimA : CreA) : (halfH ? CimB : CreB)) + (size_t)eb * 80 * DM, lw, lane);
            }
            STAMP2(14);
        } else {
            BAR();
            STAMP(8 + 12 * l + 2);
            const int fr = lane & 15, fq = lane >> 4;
            const int wm = w & 1, wn = w >> 1;
            int rdT[2], rdE[2];
            #pragma unroll
            for (int kk = 0; kk < 2; ++kk) {
                rdT[kk] = (wm * 64 + fr) * 128 + (((kk * 4 + fq) ^ ((fr >> 1) & 7)) * 16);
                rdE[kk] = L_AB + wn * 10240 + (2 * kk + (fq >> 1)) * 2560 + fr * 32 + (fq & 1) * 16;
            }
            f32x4 acc[5][4];
            #pragma unroll
            for (int i = 0; i < 5; ++i)
                #pragma unroll
                for (int jj = 0; jj < 4; ++jj) acc[i][jj] = (f32x4){0.f, 0.f, 0.f, 0.f};
            bf16x8 ft[2][4], fe[2][5];
            auto rd = [&](int kk, int slot) __attribute__((always_inline)) {
                const int wb = slot == 3 ? L_WD : L_WA + slot * 16384;
                #pragma unroll
                for (int jj = 0; jj < 4; ++jj) ft[kk][jj] = *(const LAS bf16x8*)(smem + wb + rdT[kk] + jj * 2048);
                #pragma unroll
                for (int i = 0; i < 5; ++i) fe[kk][i] = *(const LAS bf16x8*)(smem + slot * 20480 + rdE[kk] + i * 512);
            };
            auto mm = [&](int kk) __attribute__((always_inline)) {
                #pragma unroll
                for (int i = 0; i < 5; ++i)
                    #pragma unroll
                    for (int jj = 0; jj < 4; ++jj) acc[i][jj] = __builtin_amdgcn_mfma_f32_16x16x32_bf16(fe[kk][i], ft[kk][jj], acc[i][jj], 0, 0, 0);
                #pragma unroll
                for (int q = 0; q < 9; ++q) { __builtin_amdgcn_sched_group_barrier(0x8, 2, 0); __builtin_amdgcn_sched_group_barrier(0x100, 1, 0); }
                __builtin_amdgcn_sched_group_barrier(0x8, 2, 0);
            };
            BAR();
            STAMP(8 + 12 * l + 3);
            rd(0, 0);
            for (int s0 = 0; s0 < 16; s0 += 4) {
                #pragma unroll
                for (int u = 0; u < 4; ++u) {
                    rd(1, u);
                    mm(0);
                    SCHED();
                    if (u == 1 && s0 == 4 && w == 0) poll16(hflag + 128, gen + 5 + l, lane, ws);
                    BAR();
                    SCHED();
                    rd(0, (u + 1) & 3);
                    mm(1);
                    SCHED();
                }
            }
            WAIT_L(0);
            BAR();
            STAMP(8 + 12 * l + 4);
            const __amdgpu_buffer_rsrc_t rP = __builtin_amdgcn_make_buffer_rsrc((void*)(ws + WS_P + (size_t)l * P_BYTES + (size_t)kq * XN_BYTES + (size_t)(tbS * 16 + 8 * wm) * 10240), 0, 8 * 10240, 0x00020000);
            const int cc0 = 20 * e2 + 10 * wn;
            #pragma unroll
            for (int jj = 0; jj < 4; ++jj) {
                #pragma unroll
                for (int p = 0; p < 2; ++p) {
                    const f32x4 a0 = acc[2 * p][jj], a1 = acc[2 * p + 1][jj];
                    const u32x4 o = {pk_bf16(a0[0], a0[1]), pk_bf16(a0[2], a0[3]), pk_bf16(a1[0], a1[1]), pk_bf16(a1[2], a1[3])};
                    const int so = (fr >> 3) * 10240 + fq * 128 + (fr & 7) * 16;
                    ST16_SC1(rP, o, so, (2 * jj) * 10240 + (cc0 + 4 * p) * 128);
                }
            }
            #pragma unroll
            for (int jp = 0; jp < 2; ++jp) {
                const f32x4 a0 = acc[4][2 * jp], a1 = acc[4][2 * jp + 1];
                const auto r0 = __builtin_amdgcn_permlane16_swap(pk_bf16(a0[0], a0[1]), pk_bf16(a1[0], a1[1]), false, false);
                const auto r1 = __builtin_amdgcn_permlane16_swap(pk_bf16(a0[2], a0[3]), pk_bf16(a1[2], a1[3]), false, false);
                const u32x4 o = {r0[0], r1[0], r0[1], r1[1]};
                const int so = (2 * (fq & 1) + (fr >> 3)) * 10240 + (fq >> 1) * 128 + (fr & 7) * 16;
                ST16_SC1(rP, o, so, (4 * jp) * 10240 + (cc0 + 8) * 128);
            }
            STAMP2(12);
            signal_last((LAS unsigned*)(smem + L_MISC + CNT_S), (unsigned)l * 4 + 3, ws + WS_FS + ((size_t)(l * 16 + tbS) * 16 + e2 * 4 + kq) * 128, gen + 9 + l, lane);
            STAMP(8 + 12 * l + 5);
        }
        r_phase(smem, ws, out, gen, l, tbH, j, w, lane, xr STAMP_PASS);
        STAMP(8 + 12 * l + 10);
    }
    if (w0 >= 4) { WAIT_V(0); asm volatile("" :: "v"(junk)); }
    if (b == 0 && tid == 0) __hip_atomic_store((GAS u64*)(ws + WS_GEN), gen + 16, __ATOMIC_RELAXED, __HIP_MEMORY_SCOPE_AGENT);
#endif
}

extern "C" void kernel_launch(void* const* d_in, const int* in_sizes, int n_in,
                              void* d_out, int out_size, void* d_ws, size_t ws_size,
                              hipStream_t stream) {
    (void)in_sizes; (void)n_in; (void)out_size; (void)ws_size;
    (void)hipFuncSetAttribute((const void*)fnet_persistent, hipFuncAttributeMaxDynamicSharedMemorySize, LDS_BYTES);
    fnet_persistent<<<256, 512, LDS_BYTES, stream>>>(
        (const u16*)d_in[0],
        (const u16*)d_in[1], (const u16*)d_in[2], (const u16*)d_in[3], (const u16*)d_in[4],
        (const u16*)d_in[6], (const u16*)d_in[7], (const u16*)d_in[8], (const u16*)d_in[9],
        (u16*)d_out, (char*)d_ws);
}
```

```cpp
#include <hip/hip_runtime.h>
#include <hip/hip_bf16.h>
#include <stdint.h>

typedef unsigned short u16;
typedef unsigned long long u64;
typedef short bf16x8 __attribute__((ext_vector_type(8)));
typedef float f32x4 __attribute__((ext_vector_type(4)));
typedef float f32x2 __attribute__((ext_vector_type(2)));
typedef unsigned u32x4 __attribute__((ext_vector_type(4)));
typedef unsigned u32x2 __attribute__((ext_vector_type(2)));
typedef __bf16 bf16x2v __attribute__((ext_vector_type(2)));
#define LAS __attribute__((address_space(3)))
#define GAS __attribute__((address_space(1)))

namespace {

constexpr int SEQ = 2048, DM = 640;

constexpr size_t WS_GEN = 0;
constexpr size_t WS_ERR = 256;
constexpr size_t WS_FR = 4096;
constexpr size_t WS_FH = WS_FR + 4 * 256 * 128;
constexpr size_t WS_FS = WS_FH + 4 * 512 * 128;
constexpr size_t XN_BYTES = (size_t)SEQ * DM * 2;
constexpr size_t WS_XN = 1u << 20;
constexpr size_t WS_AB = WS_XN + 4 * XN_BYTES;
constexpr size_t AB_BYTES = 2 * XN_BYTES;
constexpr size_t WS_P = WS_AB + 4 * AB_BYTES;
constexpr size_t P_BYTES = 4 * XN_BYTES;
constexpr size_t WS_END = WS_P + 4 * P_BYTES;

constexpr int L_MISC = 0;
constexpr int L_AB = 4096;
constexpr int L_C = 4096;
constexpr int L_WD = 98304;
constexpr int L_WA = 114688;
constexpr int L_JUNK = 106496;
constexpr int LDS_BYTES = 163840;
constexpr int CNT_R = 0, CNT_H0 = 16, CNT_H1 = 32, CNT_S = 48, CNT_R0 = 64, STATS = 256;

#define WAIT_V(n) asm volatile("s_waitcnt vmcnt(%0)" ::"n"(n) : "memory")
#define WAIT_L(n) asm volatile("s_waitcnt lgkmcnt(%0)" ::"n"(n) : "memory")
#define BAR() do { asm volatile("" ::: "memory"); __builtin_amdgcn_s_barrier(); asm volatile("" ::: "memory"); } while (0)
#define SCHED() __builtin_amdgcn_sched_barrier(0)

__device__ __forceinline__ unsigned pk_bf16(float a, float b) {
    const f32x2 v = {a, b};
    return __builtin_bit_cast(unsigned, __builtin_convertvector(v, bf16x2v));
}
__device__ __forceinline__ float bf_lo(unsigned u) { return __builtin_bit_cast(float, u << 16); }
__device__ __forceinline__ float bf_hi(unsigned u) { return __builtin_bit_cast(float, u & 0xffff0000u); }

__device__ __forceinline__ int perm80(int r) {
    return r < 64 ? ((r >> 5) * 32 + ((r & 15) >> 2) * 8 + ((r >> 4) & 1) * 4 + (r & 3)) : r;
}

__device__ __forceinline__ float sum_mod8(float v) {
    v += __builtin_bit_cast(float, __builtin_amdgcn_update_dpp(0, __builtin_bit_cast(int, v), 0x128  , 0xf, 0xf, false));
    { const unsigned u = __builtin_bit_cast(unsigned, v); const unsigned u2 = u;
      const auto r = __builtin_amdgcn_permlane16_swap(u, u2, false, false);
      const unsigned r0 = r[0], r1 = r[1]; v = __builtin_bit_cast(float, r0) + __builtin_bit_cast(float, r1); }
    { const unsigned u = __builtin_bit_cast(unsigned, v); const unsigned u2 = u;
      const auto r = __builtin_amdgcn_permlane32_swap(u, u2, false, false);
      const unsigned r0 = r[0], r1 = r[1]; v = __builtin_bit_cast(float, r0) + __builtin_bit_cast(float, r1); }
    return v;
}

constexpr unsigned SPIN_CAP = 400000;
#define ROT_W 2
#define ROT_C 2
#define ROT_X 1
#define WARM_U 2
#define WARM_S0A 4
#define WARM_S0B 8
#define WARM_LMAX 3

__device__ __forceinline__ void poll16(const char* p  , u64 tag, int lane, char* ws) {
    const GAS u64* f = (const GAS u64*)p;
    GAS unsigned* err = (GAS unsigned*)(ws + WS_ERR);
    for (unsigned it = 0;; ++it) {
        u64 v = tag;
        if (lane < 16) v = __hip_atomic_load(f, __ATOMIC_RELAXED, __HIP_MEMORY_SCOPE_AGENT);
        if (__all(v == tag)) break;
        if ((it & 255u) == 255u) {
            if (__builtin_amdgcn_readfirstlane(__hip_atomic_load(err, __ATOMIC_RELAXED, __HIP_MEMORY_SCOPE_AGENT)) == 0x600DBADu) break;
            if (it > SPIN_CAP) { if (lane == 0) __hip_atomic_store(err, 0x600DBADu, __ATOMIC_RELAXED, __HIP_MEMORY_SCOPE_AGENT); break; }
        }
        __builtin_amdgcn_s_sleep(1);
    }
}

__device__ __forceinline__ void signal_last(LAS unsigned* cnt, unsigned last, char* flag, u64 tag, int lane) {
    asm volatile("s_waitcnt vmcnt(0)" ::: "memory");
    unsigned old = 0;
    if (lane == 0) old = __hip_atomic_fetch_add(cnt, 1u, __ATOMIC_RELAXED, __HIP_MEMORY_SCOPE_WORKGROUP);
    old = __builtin_amdgcn_readfirstlane(old);
    if (old == last && lane == 0) __hip_atomic_store((GAS u64*)flag, tag, __ATOMIC_RELAXED, __HIP_MEMORY_SCOPE_AGENT);
}

#define ST16_SC1(rs, v, voff, soff) __builtin_amdgcn_raw_buffer_store_b128((v), (rs), (voff) + (soff), 0, 16)
#define ST8_SC1(rs, v, voff, soff) __builtin_amdgcn_raw_buffer_store_b64((v), (rs), (voff) + (soff), 0, 16)
#define LD16x4_SC1(d0, d1, d2, d3, rs, voff) asm volatile("s_nop 4\n\tbuffer_load_dwordx4 %0, %4, %5, 0 offen sc1\n\tbuffer_load_dwordx4 %1, %4, %5, 0 offen offset:1024 sc1\n\t" \
    "buffer_load_dwordx4 %2, %4, %5, 0 offen offset:2048 sc1\n\tbuffer_load_dwordx4 %3, %4, %5, 0 offen offset:3072 sc1" \
    : "=&v"(d0), "=&v"(d1), "=&v"(d2), "=&v"(d3) : "v"(voff), "s"(rs) : "memory")
#define LD16x2_SC1(d0, d1, rs, voff) asm volatile("s_nop 4\n\tbuffer_load_dwordx4 %0, %2, %3, 0 offen sc1\n\tbuffer_load_dwordx4 %1, %2, %3, 0 offen offset:1024 sc1" \
    : "=&v"(d0), "=&v"(d1) : "v"(voff), "s"(rs) : "memory")
#define WAIT_VR(n, r) asm volatile("s_waitcnt vmcnt(%1)" : "+v"(r) : "n"(n) : "memory")

#define STAMP0(i) do { } while (0)
#define STAMP(i) do { } while (0)
#define STAMP2(i) do { } while (0)
#define STAMPC(i) do { } while (0)
#define STAMP_ARG
#define STAMP_PASS

__device__ __forceinline__ void load_c(LAS unsigned char* smem, const u16* C, int lw, int lane, int rot) {
#if defined(__HIP_DEVICE_COMPILE__)
    const __amdgpu_buffer_rsrc_t rC = __builtin_amdgcn_make_buffer_rsrc((void*)C, 0, 80 * DM * 2, 0x00020000);
    int vo[3];
    #pragma unroll
    for (int q = 0; q < 3; ++q) {
        const int rg = q < 2 ? lw + 4 * q : 8 + (lw & 1);
        const int row = 8 * rg + (lane >> 3);
        vo[q] = perm80(row) * (DM * 2) + (((lane & 7) ^ ((row >> 1) & 7)) * 16);
    }
    #pragma unroll
    for (int k0 = 0; k0 < 10; ++k0) {
        int ks = k0 + rot; ks = ks >= 10 ? ks - 10 : ks;
        __builtin_amdgcn_raw_ptr_buffer_load_lds(rC, (LAS void*)(smem + L_C + ks * 10240 + lw * 1024), 16, vo[0], ks * 128, 0, 0);
        __builtin_amdgcn_raw_ptr_buffer_load_lds(rC, (LAS void*)(smem + L_C + ks * 10240 + (lw + 4) * 1024), 16, vo[1], ks * 128, 0, 0);
        if ((k0 & 1) == (lw >> 1))
            __builtin_amdgcn_raw_ptr_buffer_load_lds(rC, (LAS void*)(smem + L_C + ks * 10240 + (8 + (lw & 1)) * 1024), 16, vo[2], ks * 128, 0, 0);
    }
#endif
}
__device__ __forceinline__ void warm_w(LAS unsigned char* smem, const u16* W, int e2, int lw, int lane) {
#if defined(__HIP_DEVICE_COMPILE__)
    const __amdgpu_buffer_rsrc_t rW = __builtin_amdgcn_make_buffer_rsrc((void*)W, 0, 128 * SEQ * 2, 0x00020000);
    const int vo = (32 * e2 + 8 * lw + (lane >> 3)) * (SEQ * 2) + (lane & 7) * 16;
    const unsigned dst = (unsigned)(size_t)smem + L_JUNK + lw * 1024;
    #pragma unroll
    for (int q = 0; q < 16; ++q) {
        unsigned keep;
        asm volatile("s_mov_b32 %0, m0\n\ts_mov_b32 m0, %2\n\ts_nop 4\n\tbuffer_load_dwordx4 %1, %3, %4 offen lds\n\ts_mov_b32 m0, %0"
                     : "=&s"(keep) : "v"(vo), "s"(dst), "s"(rW), "s"(q * 128) : "memory");
    }
#endif
}

__device__ __forceinline__ void r_first(LAS unsigned char* smem, char* ws, const u16* x_in, u64 gen, int tbH, int j, int w, int lane) {
#if defined(__HIP_DEVICE_COMPILE__)
    float v[24];
    const bool third = (lane >> 5) == (w & 1);
    const int slot2 = 8 + (w >> 1);
    if (w < 4) {
        const u16* xp = x_in + (size_t)(tbH * 128 + j * 8 + (lane & 7)) * DM + (lane >> 3) * 8;
        u32x4 x[3];
        x[0] = *(const u32x4*)(xp + w * 64); x[1] = *(const u32x4*)(xp + (w + 4) * 64);
        x[2] = (u32x4){0, 0, 0, 0};
        if (third) x[2] = *(const u32x4*)(xp + slot2 * 64);
        #pragma unroll
        for (int c = 0; c < 3; ++c)
            #pragma unroll
            for (int q = 0; q < 4; ++q) { v[c * 8 + 2 * q] = bf_lo(x[c][q]); v[c * 8 + 2 * q + 1] = bf_hi(x[c][q]); }
        float s = 0.f;
        #pragma unroll
        for (int q = 0; q < 24; ++q) s += v[q];
        const float mean_w = sum_mod8(s) * (1.0f / 160.0f);
        float m2 = 0.f, m3 = 0.f;
        #pragma unroll
        for (int q = 0; q < 16; ++q) { const float d = v[q] - mean_w; m2 += d * d; }
        #pragma unroll
        for (int q = 16; q < 24; ++q) { const float d = v[q] - mean_w; m3 += d * d; }
        if (third) m2 += m3;
        const float m2_w = sum_mod8(m2);
        if (lane < 8) *(LAS f32x2*)(smem + L_MISC + STATS + (lane * 4 + w) * 8) = (f32x2){mean_w, m2_w};
    }
    WAIT_L(0);
    BAR();
    if (w < 4) {
        const LAS f32x4* sp = (const LAS f32x4*)(smem + L_MISC + STATS + (lane & 7) * 32);
        const f32x4 s0 = sp[0], s1 = sp[1];
        const float mean = ((s0[0] + s0[2]) + (s1[0] + s1[2])) * 0.25f;
        float m2 = (s0[1] + s0[3]) + (s1[1] + s1[3]);
        { const float d0 = s0[0] - mean, d1 = s0[2] - mean, d2 = s1[0] - mean, d3 = s1[2] - mean; m2 += 160.0f * ((d0 * d0 + d1 * d1) + (d2 * d2 + d3 * d3)); }
        const float rstd = 1.0f / sqrtf(m2 * (1.0f / DM) + 1e-5f);
        const __amdgpu_buffer_rsrc_t rX = __builtin_amdgcn_make_buffer_rsrc((void*)(ws + WS_XN + (size_t)tbH * 163840 + (j >> 1) * 20480), 0, 20480, 0x00020000);
        const int vo = (lane >> 5) * 1024 + ((lane >> 3) & 3) * 256 + ((j & 1) * 8 + (lane & 7)) * 16;
        #pragma unroll
        for (int c = 0; c < 3; ++c) {
            u32x4 o;
            #pragma unroll
            for (int q = 0; q < 4; ++q) o[q] = pk_bf16((v[c * 8 + 2 * q] - mean) * rstd, (v[c * 8 + 2 * q + 1] - mean) * rstd);
            if (c == 0) ST16_SC1(rX, o, vo, w * 2048);
            else if (c == 1) ST16_SC1(rX, o, vo, (w + 4) * 2048);
            else if (third) ST16_SC1(rX, o, vo, slot2 * 2048);
        }
        signal_last((LAS unsigned*)(smem + L_MISC + CNT_R0), 3u, ws + WS_FR + ((size_t)tbH * 16 + j) * 128, gen + 1, lane);
    }
#endif
}

__device__ __forceinline__ void r_phase(LAS unsigned char* smem, char* ws, u16* out, u64 gen, int l, int tbH, int j, int w, int lane, u32x4 (&xr)[2] STAMP_ARG) {
#if defined(__HIP_DEVICE_COMPILE__)
    if (w == 0) poll16(ws + WS_FS + ((size_t)(l * 16 + tbH) * 16 + (lane & 15)) * 128, gen + 9 + l, lane, ws);
    BAR();
    STAMP(8 + 12 * l + 9);
    float v[16];
    const bool second = (lane >> 4) == (w & 3);
    const int slot2 = 8 + (w >> 2);
    {
        const __amdgpu_buffer_rsrc_t rP = __builtin_amdgcn_make_buffer_rsrc((void*)(ws + WS_P + (size_t)l * P_BYTES + (size_t)(tbH * 16 + j) * 10240), 0, (int)(3 * XN_BYTES + 10240), 0x00020000);
        u32x4 p[4][2];
        const int vo = lane * 16 + w * 1024;
        const int vo2 = second ? lane * 16 + slot2 * 1024 : 0x7ffffff0;
        #pragma unroll
        for (int q = 0; q < 4; ++q)
            asm volatile("s_nop 4\n\tbuffer_load_dwordx4 %0, %2, %4, %5 offen sc1\n\tbuffer_load_dwordx4 %1, %3, %4, %5 offen sc1"
                         : "=&v"(p[q][0]), "=&v"(p[q][1]) : "v"(vo), "v"(vo2), "s"(rP), "s"(q * (int)XN_BYTES) : "memory");
        STAMP2(20);
        asm volatile("s_waitcnt vmcnt(0)" : "+v"(p[0][0]), "+v"(p[0][1]), "+v"(p[1][0]), "+v"(p[1][1]), "+v"(p[2][0]), "+v"(p[2][1]), "+v"(p[3][0]), "+v"(p[3][1]) :: "memory");
        STAMP2(21);
        #pragma unroll
        for (int c = 0; c < 2; ++c)
            #pragma unroll
            for (int q = 0; q < 4; ++q) {
                const float lo = bf_lo(xr[c][q]) + ((bf_lo(p[0][c][q]) + bf_lo(p[1][c][q])) + (bf_lo(p[2][c][q]) + bf_lo(p[3][c][q])));
                const float hi = bf_hi(xr[c][q]) + ((bf_hi(p[0][c][q]) + bf_hi(p[1][c][q])) + (bf_hi(p[2][c][q]) + bf_hi(p[3][c][q])));
                const unsigned u = pk_bf16(lo, hi);
                xr[c][q] = u; v[c * 8 + 2 * q] = bf_lo(u); v[c * 8 + 2 * q + 1] = bf_hi(u);
            }
    }
    if (l == 3) {
        u16* op = out + (size_t)(tbH * 128 + j * 8 + (lane & 7)) * DM + (lane >> 3) * 8;
        *(u32x4*)(op + w * 64) = xr[0];
        if (second) *(u32x4*)(op + slot2 * 64) = xr[1];
        return;
    }
    {
        float s = 0.f;
        #pragma unroll
        for (int q = 0; q < 16; ++q) s += v[q];
        const float mean_w = sum_mod8(s) * (1.0f / 80.0f);
        float m2 = 0.f, m3 = 0.f;
        #pragma unroll
        for (int q = 0; q < 8; ++q) { const float d = v[q] - mean_w; m2 += d * d; }
        #pragma unroll
        for (int q = 8; q < 16; ++q) { const float d = v[q] - mean_w; m3 += d * d; }
        if (second) m2 += m3;
        const float m2_w = sum_mod8(m2);
        if (lane < 8) *(LAS f32x2*)(smem + L_MISC + STATS + (lane * 8 + w) * 8) = (f32x2){mean_w, m2_w};
    }
    STAMP2(22);
    WAIT_L(0);
    BAR();
    STAMP2(23);
    {
        const int ln = l + 1;
        const LAS f32x4* sp = (const LAS f32x4*)(smem + L_MISC + STATS + (lane & 7) * 64);
        const f32x4 s0 = sp[0], s1 = sp[1], s2 = sp[2], s3 = sp[3];
        const float mean = (((s0[0] + s0[2]) + (s1[0] + s1[2])) + ((s2[0] + s2[2]) + (s3[0] + s3[2]))) * 0.125f;
        float m2 = ((s0[1] + s0[3]) + (s1[1] + s1[3])) + ((s2[1] + s2[3]) + (s3[1] + s3[3]));
        { const float d0 = s0[0] - mean, d1 = s0[2] - mean, d2 = s1[0] - mean, d3 = s1[2] - mean, d4 = s2[0] - mean, d5 = s2[2] - mean, d6 = s3[0] - mean, d7 = s3[2] - mean;
          m2 += 80.0f * (((d0 * d0 + d1 * d1) + (d2 * d2 + d3 * d3)) + ((d4 * d4 + d5 * d5) + (d6 * d6 + d7 * d7))); }
        const float rstd = 1.0f / sqrtf(m2 * (1.0f / DM) + 1e-5f);
        const __amdgpu_buffer_rsrc_t rX = __builtin_amdgcn_make_buffer_rsrc((void*)(ws + WS_XN + (size_t)ln * XN_BYTES + (size_t)tbH * 163840 + (j >> 1) * 20480), 0, 20480, 0x00020000);
        const int vo = (lane >> 5) * 1024 + ((lane >> 3) & 3) * 256 + ((j & 1) * 8 + (lane & 7)) * 16;
        #pragma unroll
        for (int c = 0; c < 2; ++c) {
            u32x4 o;
            #pragma unroll
            for (int q = 0; q < 4; ++q) o[q] = pk_bf16((v[c * 8 + 2 * q] - mean) * rstd, (v[c * 8 + 2 * q + 1] - mean) * rstd);
            if (c == 0) ST16_SC1(rX, o, vo, w * 2048);
            else if (second) ST16_SC1(rX, o, vo, slot2 * 2048);
        }
        STAMP2(24);
        signal_last((LAS unsigned*)(smem + L_MISC + CNT_R), (unsigned)l * 8 + 7, ws + WS_FR + ((size_t)(ln * 16 + tbH) * 16 + j) * 128, gen + 1 + ln, lane);
    }
#endif
}

}

__global__ __launch_bounds__(512, 2) void fnet_persistent(const u16* __restrict__ x_in,
        const u16* __restrict__ WreA, const u16* __restrict__ WimA, const u16* __restrict__ CreA, const u16* __restrict__ CimA,
        const u16* __restrict__ WreB, const u16* __restrict__ WimB, const u16* __restrict__ CreB, const u16* __restrict__ CimB,
        u16* __restrict__ out, char* __restrict__ ws) {
#if defined(__HIP_DEVICE_COMPILE__)
    extern __shared__ __attribute__((aligned(16))) unsigned char smem_[];
    LAS unsigned char* smem = (LAS unsigned char*)smem_;
    const int tid = threadIdx.x;
    int lane = tid & 63;
    const int w0 = __builtin_amdgcn_readfirstlane(tid >> 6);
    const int b = blockIdx.x;
#define GEOM(bb) const int xcd = (bb) & 7, idx = (bb) >> 3; \
    const int kq = xcd & 3, tbS = (xcd >> 2) * 8 + (idx >> 2), e2 = idx & 3; \
    const int tbH = (xcd >> 1) * 4 + (idx >> 3), j = (xcd & 1) * 8 + (idx & 7); \
    const int halfS = kq >> 1, halfH = j >> 3, eb = j & 7; (void)halfS; (void)halfH; (void)eb; (void)e2; (void)tbS;

    STAMP0(0);
    u64 gen = __hip_atomic_load((const GAS u64*)(ws + WS_GEN), __ATOMIC_RELAXED, __HIP_MEMORY_SCOPE_AGENT);
    gen = ((u64)(unsigned)__builtin_amdgcn_readfirstlane((unsigned)(gen >> 32)) << 32) | (unsigned)__builtin_amdgcn_readfirstlane((unsigned)gen);
    if (tid < 64) *(LAS unsigned*)(smem + L_MISC + tid * 4) = 0;
    WAIT_L(0);
    BAR();

    u32x4 xr[2];
    xr[0] = (u32x4){0, 0, 0, 0}; xr[1] = xr[0];
    {
        GEOM(b)
        const int w = w0;
        if (w >= 4) {
            __builtin_amdgcn_s_sleep(8);
            load_c(smem, (halfH ? CimA : CreA) + (size_t)eb * 80 * DM, w - 4, lane, ROT_C * (tbH & 3));
        }
        STAMP(1);
        r_first(smem, ws, x_in, gen, tbH, j, w, lane);
        {
            const u16* xp = x_in + (size_t)(tbH * 128 + j * 8 + (lane & 7)) * DM + (lane >> 3) * 8;
            xr[0] = *(const u32x4*)(xp + w * 64);
            if ((lane >> 4) == (w & 3)) xr[1] = *(const u32x4*)(xp + (8 + (w >> 2)) * 64);
        }
    }
    STAMP(2);

    for (int l = 0; l < 4; ++l) {
        const int set = l & 1;
        asm volatile("" : "+v"(lane));
        int bb = b; asm volatile("" : "+s"(bb));
        int w = w0; asm volatile("" : "+s"(w));
        GEOM(bb)
        if (w >= 4) {
            WAIT_V(0);
            if (w == 4) poll16(ws + WS_FR + ((size_t)(l * 16 + tbH) * 16 + (lane & 15)) * 128, gen + 1 + l, lane, ws);
        }
        BAR();
        STAMP(8 + 12 * l + 0);
        STAMPC(48 + 2 * l);
        {
            const int g = w ^ 4;
            if (w >= 4) __builtin_amdgcn_s_setprio(2);
            const __amdgpu_buffer_rsrc_t rX = __builtin_amdgcn_make_buffer_rsrc((void*)(ws + WS_XN + (size_t)l * XN_BYTES + (size_t)tbH * 163840 + g * 20480), 0, 20480, 0x00020000);
            u32x4 xf[20];
            const int vo = lane * 16;
            const int rot = ROT_X * (j & 7);
            #pragma unroll
            for (int q = 0; q < 10; ++q) { int k2 = q + rot; k2 = k2 >= 10 ? k2 - 10 : k2; LD16x2_SC1(xf[2 * q], xf[2 * q + 1], rX, vo + k2 * 2048); }
            STAMP2(1);
            const int fr = lane & 15, fq = lane >> 4;
            int rdC[2];
            #pragma unroll
            for (int kk = 0; kk < 2; ++kk) rdC[kk] = L_C + fr * 128 + (((4 * kk + fq) ^ ((fr >> 1) & 7)) * 16);
            f32x4 acc[5];
            #pragma unroll
            for (int i = 0; i < 5; ++i) acc[i] = (f32x4){0.f, 0.f, 0.f, 0.f};
            bf16x8 cf[2][5];
            #pragma unroll
            for (int i = 0; i < 5; ++i) cf[0][i] = *(const LAS bf16x8*)(smem + rdC[0] + rot * 10240 + i * 2048);
            #pragma unroll
            for (int ks = 0; ks < 20; ++ks) {
                if (ks + 1 < 20) {
                    int k2 = ((ks + 1) >> 1) + rot; k2 = k2 >= 10 ? k2 - 10 : k2;
                    #pragma unroll
                    for (int i = 0; i < 5; ++i) cf[(ks + 1) & 1][i] = *(const LAS bf16x8*)(smem + rdC[(ks + 1) & 1] + k2 * 10240 + i * 2048);
                }
                WAIT_VR(19 - ks, xf[ks]);
                if (ks == 0) STAMP2(2);
                if (ks == 10) STAMP2(3);
                if (ks == 19) STAMP2(4);
                #pragma unroll
                for (int i = 0; i < 5; ++i) acc[i] = __builtin_amdgcn_mfma_f32_16x16x32_bf16(__builtin_bit_cast(bf16x8, xf[ks]), cf[ks & 1][i], acc[i], 0, 0, 0);
            }
            STAMP2(5);
            const float sg = halfH ? -1.0f : 1.0f;
            unsigned d[5][2];
            #pragma unroll
            for (int i = 0; i < 5; ++i) { d[i][0] = pk_bf16(acc[i][0] * sg, acc[i][1] * sg); d[i][1] = pk_bf16(acc[i][2] * sg, acc[i][3] * sg); }
            const int sigma = 2 * tbH + (g >> 2);
            const __amdgpu_buffer_rsrc_t rA = __builtin_amdgcn_make_buffer_rsrc((void*)(ws + WS_AB + (size_t)l * AB_BYTES + ((size_t)(halfH * 4 + (eb >> 1)) * 32 + sigma) * 20480 + (eb & 1) * 10240 + (g & 3) * 2560), 0, 2560, 0x00020000);
            #pragma unroll
            for (int p = 0; p < 2; ++p) {
                const auto r0 = __builtin_amdgcn_permlane16_swap(d[2 * p][0], d[2 * p + 1][0], false, false);
                const auto r1 = __builtin_amdgcn_permlane16_swap(d[2 * p][1], d[2 * p + 1][1], false, false);
                const u32x4 o = {r0[0], r1[0], r0[1], r1[1]};
                const int so = (16 * (fq & 1) + fr) * 32 + (fq >> 1) * 16;
                ST16_SC1(rA, o, so, p * 1024);
            }
            { const u32x2 o = {d[4][0], d[4][1]}; ST8_SC1(rA, o, fr * 32 + fq * 8, 2048); }
            STAMP2(6);
            signal_last((LAS unsigned*)(smem + L_MISC + (w >= 4 ? CNT_H0 : CNT_H1)), (unsigned)l * 4 + 3,
                        ws + WS_FH + (((size_t)(l * 16 + tbH) * 16 + j) * 2 + (g >> 2)) * 128, gen + 5 + l, lane);
            if (w >= 4) __builtin_amdgcn_s_setprio(0);
        }
        STAMP(8 + 12 * l + 1);
        STAMPC(49 + 2 * l);
        const char* hflag = ws + WS_FH + (((size_t)(l * 16 + 8 * (kq & 1) + ((lane & 15) >> 1)) * 16 + 8 * halfS + 2 * e2 + (lane & 1)) * 2) * 128;
        if (w >= 4) {
            const int lw = w - 4;
            const u16* W = set ? (halfS ? WimB : WreB) : (halfS ? WimA : WreA);
            const __amdgpu_buffer_rsrc_t rW = __builtin_amdgcn_make_buffer_rsrc((void*)(W + (size_t)tbS * 128 * SEQ + (kq & 1) * 1024), 0, 128 * SEQ * 2, 0x00020000);
            const __amdgpu_buffer_rsrc_t rA = __builtin_amdgcn_make_buffer_rsrc((void*)(ws + WS_AB + (size_t)l * AB_BYTES + ((size_t)(halfS * 4 + e2) * 32 + (kq & 1) * 16) * 20480), 0, 16 * 20480, 0x00020000);
            const int voffW = (lane >> 3) * (SEQ * 2) + (((lane & 7) ^ ((4 * (lw & 1) + (lane >> 4)) & 7)) * 16);
            const int voffA = lane * 16;
            auto sig = [&](int x) __attribute__((always_inline)) { return 2 * ((x + tbS + ROT_W * e2) & 7) + (x >> 3); };
            auto issue_w = [&](int x) __attribute__((always_inline)) {
                const int sg_ = sig(x), dst = (x & 3) == 3 ? L_WD : L_WA + (x & 3) * 16384;
                #pragma unroll
                for (int q = 0; q < 4; ++q) { const int i = lw + 4 * q;
                    __builtin_amdgcn_raw_ptr_buffer_load_lds(rW, (LAS void*)(smem + dst + i * 1024), 16, voffW, sg_ * 128 + i * 8 * (SEQ * 2), 0, 0); }
            };
            auto issue_a = [&](int x) __attribute__((always_inline)) {
                const int sg_ = sig(x), dst = L_AB + (x & 3) * 20480;
                #pragma unroll
                for (int q = 0; q < 5; ++q) { const int i = lw + 4 * q;
                    __builtin_amdgcn_raw_ptr_buffer_load_lds(rA, (LAS void*)(smem + dst + i * 1024), 16, voffA, sg_ * 20480 + i * 1024, 0, 16); }
            };
            issue_w(0); issue_w(1); issue_w(2);
            if (w == 4) poll16(hflag, gen + 5 + l, lane, ws);
            BAR();
            STAMP(8 + 12 * l + 2);
            issue_a(0); issue_a(1);
            WAIT_V(5);
            BAR();
            STAMP(8 + 12 * l + 3);
            issue_a(2); issue_w(3);
            WAIT_V(9);  BAR(); issue_a(3);
            WAIT_V(9);  BAR(); issue_w(4); issue_a(4);
            for (int st = 2; st < 13; ++st) { WAIT_V(9); BAR(); issue_w(st + 3); issue_a(st + 3); }
            WAIT_V(9); BAR();
            WAIT_V(0); BAR();
            BAR();
            BAR();
            STAMP(8 + 12 * l + 4);
            STAMP2(13);
            if (l < 3) {
                volatile LAS unsigned* cs = (volatile LAS unsigned*)(smem + L_MISC + CNT_S);
                for (unsigned it = 0; it < 2000000u && (unsigned)__builtin_amdgcn_readfirstlane(*cs) < 4u * (unsigned)(l + 1); ++it) __builtin_amdgcn_s_sleep(2);
                load_c(smem, (set ? (halfH ? CimA : CreA) : (halfH ? CimB : CreB)) + (size_t)eb * 80 * DM, lw, lane, ROT_C * (tbH & 3));
            }
            STAMP2(14);
        } else {
            BAR();
            STAMP(8 + 12 * l + 2);
            const int fr = lane & 15, fq = lane >> 4;
            const int wm = w & 1, wn = w >> 1;
            int rdT[2], rdE[2];
            #pragma unroll
            for (int kk = 0; kk < 2; ++kk) {
                rdT[kk] = (wm * 64 + fr) * 128 + (((kk * 4 + fq) ^ ((fr >> 1) & 7)) * 16);
                rdE[kk] = L_AB + wn * 10240 + (2 * kk + (fq >> 1)) * 2560 + fr * 32 + (fq & 1) * 16;
            }
            f32x4 acc[5][4];
            #pragma unroll
            for (int i = 0; i < 5; ++i)
                #pragma unroll
                for (int jj = 0; jj < 4; ++jj) acc[i][jj] = (f32x4){0.f, 0.f, 0.f, 0.f};
            bf16x8 ft[2][4], fe[2][5];
            auto rd = [&](int kk, int slot) __attribute__((always_inline)) {
                const int wb = slot == 3 ? L_WD : L_WA + slot * 16384;
                #pragma unroll
                for (int jj = 0; jj < 4; ++jj) ft[kk][jj] = *(const LAS bf16x8*)(smem + wb + rdT[kk] + jj * 2048);
                #pragma unroll
                for (int i = 0; i < 5; ++i) fe[kk][i] = *(const LAS bf16x8*)(smem + slot * 20480 + rdE[kk] + i * 512);
            };
            auto mm = [&](int kk) __attribute__((always_inline)) {
                #pragma unroll
                for (int i = 0; i < 5; ++i)
                    #pragma unroll
                    for (int jj = 0; jj < 4; ++jj) acc[i][jj] = __builtin_amdgcn_mfma_f32_16x16x32_bf16(fe[kk][i], ft[kk][jj], acc[i][jj], 0, 0, 0);
                #pragma unroll
                for (int q = 0; q < 9; ++q) { __builtin_amdgcn_sched_group_barrier(0x8, 2, 0); __builtin_amdgcn_sched_group_barrier(0x100, 1, 0); }
                __builtin_amdgcn_sched_group_barrier(0x8, 2, 0);
            };
            BAR();
            STAMP(8 + 12 * l + 3);
            STAMPC(40 + 2 * l);
            rd(0, 0);
            for (int s0 = 0; s0 < 16; s0 += 4) {
                #pragma unroll
                for (int u = 0; u < 4; ++u) {
                    rd(1, u);
                    mm(0);
                    SCHED();
                    if (u == 1 && s0 == 4 && w == 0) poll16(hflag + 128, gen + 5 + l, lane, ws);
                    BAR();
                    SCHED();
                    rd(0, (u + 1) & 3);
                    mm(1);
                    SCHED();
                }
            }
            WAIT_L(0);
            BAR();
            STAMP(8 + 12 * l + 4);
            STAMPC(41 + 2 * l);
            const __amdgpu_buffer_rsrc_t rP = __builtin_amdgcn_make_buffer_rsrc((void*)(ws + WS_P + (size_t)l * P_BYTES + (size_t)kq * XN_BYTES + (size_t)(tbS * 16 + 8 * wm) * 10240), 0, 8 * 10240, 0x00020000);
            const int cc0 = 20 * e2 + 10 * wn;
            #pragma unroll
            for (int jj = 0; jj < 4; ++jj) {
                #pragma unroll
                for (int p = 0; p < 2; ++p) {
                    const f32x4 a0 = acc[2 * p][jj], a1 = acc[2 * p + 1][jj];
                    const u32x4 o = {pk_bf16(a0[0], a0[1]), pk_bf16(a0[2], a0[3]), pk_bf16(a1[0], a1[1]), pk_bf16(a1[2], a1[3])};
                    const int so = (fr >> 3) * 10240 + fq * 128 + (fr & 7) * 16;
                    ST16_SC1(rP, o, so, (2 * jj) * 10240 + (cc0 + 4 * p) * 128);
                }
            }
            #pragma unroll
            for (int jp = 0; jp < 2; ++jp) {
                const f32x4 a0 = acc[4][2 * jp], a1 = acc[4][2 * jp + 1];
                const auto r0 = __builtin_amdgcn_permlane16_swap(pk_bf16(a0[0], a0[1]), pk_bf16(a1[0], a1[1]), false, false);
                const auto r1 = __builtin_amdgcn_permlane16_swap(pk_bf16(a0[2], a0[3]), pk_bf16(a1[2], a1[3]), false, false);
                const u32x4 o = {r0[0], r1[0], r0[1], r1[1]};
                const int so = (2 * (fq & 1) + (fr >> 3)) * 10240 + (fq >> 1) * 128 + (fr & 7) * 16;
                ST16_SC1(rP, o, so, (4 * jp) * 10240 + (cc0 + 8) * 128);
            }
            STAMP2(12);
            signal_last((LAS unsigned*)(smem + L_MISC + CNT_S), (unsigned)l * 4 + 3, ws + WS_FS + ((size_t)(l * 16 + tbS) * 16 + e2 * 4 + kq) * 128, gen + 9 + l, lane);
            STAMP(8 + 12 * l + 5);
        }
        r_phase(smem, ws, out, gen, l, tbH, j, w, lane, xr STAMP_PASS);
        STAMP(8 + 12 * l + 10);
    }
    WAIT_V(0);
    if (b == 0 && tid == 0) __hip_atomic_store((GAS u64*)(ws + WS_GEN), gen + 16, __ATOMIC_RELAXED, __HIP_MEMORY_SCOPE_AGENT);
#endif
}

extern "C" void kernel_launch(void* const* d_in, const int* in_sizes, int n_in,
                              void* d_out, int out_size, void* d_ws, size_t ws_size,
                              hipStream_t stream) {
    (void)in_sizes; (void)n_in; (void)out_size; (void)ws_size;
    (void)hipFuncSetAttribute((const void*)fnet_persistent, hipFuncAttributeMaxDynamicSharedMemorySize, LDS_BYTES);
    fnet_persistent<<<256, 512, LDS_BYTES, stream>>>(
        (const u16*)d_in[0],
        (const u16*)d_in[1], (const u16*)d_in[2], (const u16*)d_in[3], (const u16*)d_in[4],
        (const u16*)d_in[6], (const u16*)d_in[7], (const u16*)d_in[8], (const u16*)d_in[9],
        (u16*)d_out, (char*)d_ws);
}
```

```cpp
#include <hip/hip_runtime.h>
#include <hip/hip_bf16.h>
#include <stdint.h>

typedef unsigned short u16;
typedef unsigned long long u64;
typedef short bf16x8 __attribute__((ext_vector_type(8)));
typedef float f32x4 __attribute__((ext_vector_type(4)));
typedef float f32x2 __attribute__((ext_vector_type(2)));
typedef unsigned u32x4 __attribute__((ext_vector_type(4)));
typedef unsigned u32x2 __attribute__((ext_vector_type(2)));
typedef __bf16 bf16x2v __attribute__((ext_vector_type(2)));
#define LAS __attribute__((address_space(3)))
#define GAS __attribute__((address_space(1)))

namespace {

constexpr int SEQ = 2048, DM = 640;

constexpr size_t WS_GEN = 0;
constexpr size_t WS_ERR = 256;
constexpr size_t WS_FX = 4096;
constexpr size_t WS_FH = WS_FX + 4 * 256 * 128;
constexpr size_t WS_FS = WS_FH + 4 * 512 * 128;
constexpr size_t WS_ST = 1u << 20;
constexpr size_t XN_BYTES = (size_t)SEQ * DM * 2;
constexpr size_t WS_XN = 2u << 20;
constexpr size_t WS_AB = WS_XN + 4 * XN_BYTES;
constexpr size_t AB_BYTES = 2 * XN_BYTES;
constexpr size_t WS_P = WS_AB + 4 * AB_BYTES;
constexpr size_t P_BYTES = 4 * XN_BYTES;
constexpr size_t WS_END = WS_P + 4 * P_BYTES;

constexpr int L_MISC = 0;
constexpr int L_C = 4096;
__device__ __forceinline__ constexpr int w_slot(int s) { return s == 0 ? 126976 : s == 1 ? 143360 : s == 2 ? 65536 : 81920; }
__device__ __forceinline__ constexpr int a_slot(int s) { return s == 0 ? 106496 : s == 1 ? 4096 : s == 2 ? 24576 : 45056; }
constexpr int LDS_BYTES = 163840;
constexpr int CNT_C = 0, CNT_H0 = 16, CNT_H1 = 32, CNT_GO = 48, CNT_PUB = 64, CNT_CS = 80, CNT_E = 96, CNT_X = 112, CSUM = 256;

#define WAIT_V(n) asm volatile("s_waitcnt vmcnt(%0)" ::"n"(n) : "memory")
#define WAIT_L(n) asm volatile("s_waitcnt lgkmcnt(%0)" ::"n"(n) : "memory")
#define BAR() do { asm volatile("" ::: "memory"); __builtin_amdgcn_s_barrier(); asm volatile("" ::: "memory"); } while (0)
#define SCHED() __builtin_amdgcn_sched_barrier(0)

__device__ __forceinline__ unsigned pk_bf16(float a, float b) {
    const f32x2 v = {a, b};
    return __builtin_bit_cast(unsigned, __builtin_convertvector(v, bf16x2v));
}
__device__ __forceinline__ float bf_lo(unsigned u) { return __builtin_bit_cast(float, u << 16); }
__device__ __forceinline__ float bf_hi(unsigned u) { return __builtin_bit_cast(float, u & 0xffff0000u); }

__device__ __forceinline__ int perm80(int r) {
    return r < 64 ? ((r >> 5) * 32 + ((r & 15) >> 2) * 8 + ((r >> 4) & 1) * 4 + (r & 3)) : r;
}

__device__ __forceinline__ float sum_mod16(float v) {
    { const unsigned u = __builtin_bit_cast(unsigned, v); const unsigned u2 = u;
      const auto r = __builtin_amdgcn_permlane16_swap(u, u2, false, false);
      const unsigned r0 = r[0], r1 = r[1]; v = __builtin_bit_cast(float, r0) + __builtin_bit_cast(float, r1); }
    { const unsigned u = __builtin_bit_cast(unsigned, v); const unsigned u2 = u;
      const auto r = __builtin_amdgcn_permlane32_swap(u, u2, false, false);
      const unsigned r0 = r[0], r1 = r[1]; v = __builtin_bit_cast(float, r0) + __builtin_bit_cast(float, r1); }
    return v;
}

__device__ __forceinline__ unsigned lds_peek(unsigned addr) {
    unsigned v;
    asm volatile("ds_read_b32 %0, %1\n\ts_waitcnt lgkmcnt(0)" : "=&v"(v) : "v"(addr) : "memory");
    return __builtin_amdgcn_readfirstlane(v);
}
__device__ __forceinline__ void lds_inc(unsigned addr) {
    const unsigned one = 1;
    asm volatile("s_mov_b64 exec, 1\n\tds_add_u32 %0, %1\n\ts_mov_b64 exec, -1" :: "v"(addr), "v"(one) : "memory");
}
constexpr unsigned SPIN_CAP = 400000;
__device__ __forceinline__ bool spin_over(unsigned it, int lane, char* ws) {
    if ((it & 255u) != 255u) return false;
    GAS unsigned* err = (GAS unsigned*)(ws + WS_ERR);
    if (__builtin_amdgcn_readfirstlane(__hip_atomic_load(err, __ATOMIC_RELAXED, __HIP_MEMORY_SCOPE_AGENT)) == 0x600DBADu) return true;
    if (it > SPIN_CAP) { if (lane == 0) __hip_atomic_store(err, 0x600DBADu, __ATOMIC_RELAXED, __HIP_MEMORY_SCOPE_AGENT); return true; }
    return false;
}
#define W_AUX 0
#define A_AUX 16
#define CPRE 3
#define XPRE 3
#define XINC 4
#define ROT_W 2
#define ROT_C 2
#define ROT_X 1

__device__ __forceinline__ void poll_n(const char* p  , int n, u64 tag, int lane, char* ws) {
    const GAS u64* f = (const GAS u64*)p;
    for (unsigned it = 0;; ++it) {
        u64 v = tag;
        if (lane < n) v = __hip_atomic_load(f, __ATOMIC_RELAXED, __HIP_MEMORY_SCOPE_AGENT);
        if (__all(v == tag) || spin_over(it, lane, ws)) break;
        __builtin_amdgcn_s_sleep(1);
    }
}

__device__ __forceinline__ void signal_last(LAS unsigned* cnt, unsigned last, char* flag, u64 tag, int lane) {
    asm volatile("s_waitcnt vmcnt(0)" ::: "memory");
    unsigned old = 0;
    if (lane == 0) old = __hip_atomic_fetch_add(cnt, 1u, __ATOMIC_RELAXED, __HIP_MEMORY_SCOPE_WORKGROUP);
    old = __builtin_amdgcn_readfirstlane(old);
    if (old == last && lane == 0) __hip_atomic_store((GAS u64*)flag, tag, __ATOMIC_RELAXED, __HIP_MEMORY_SCOPE_AGENT);
}

#define ST16_SC1(rs, v, voff, soff) __builtin_amdgcn_raw_buffer_store_b128((v), (rs), (voff) + (soff), 0, 16)
#define ST8_SC1(rs, v, voff, soff) __builtin_amdgcn_raw_buffer_store_b64((v), (rs), (voff) + (soff), 0, 16)
#define LD16x4_SC1(d0, d1, d2, d3, rs, voff) asm volatile("s_nop 4\n\tbuffer_load_dwordx4 %0, %4, %5, 0 offen sc1\n\tbuffer_load_dwordx4 %1, %4, %5, 0 offen offset:1024 sc1\n\t" \
    "buffer_load_dwordx4 %2, %4, %5, 0 offen offset:2048 sc1\n\tbuffer_load_dwordx4 %3, %4, %5, 0 offen offset:3072 sc1" \
    : "=&v"(d0), "=&v"(d1), "=&v"(d2), "=&v"(d3) : "v"(voff), "s"(rs) : "memory")
#define LD16x2_SC1(d0, d1, rs, voff) asm volatile("s_nop 4\n\tbuffer_load_dwordx4 %0, %2, %3, 0 offen sc1\n\tbuffer_load_dwordx4 %1, %2, %3, 0 offen offset:1024 sc1" \
    : "=&v"(d0), "=&v"(d1) : "v"(voff), "s"(rs) : "memory")
#define GLDS_ASM(rs, voff, soff, ldsaddr) do { unsigned keep_; \
    asm volatile("s_mov_b32 %0, m0\n\ts_mov_b32 m0, %2\n\ts_nop 4\n\tbuffer_load_dwordx4 %1, %3, %4 offen lds\n\ts_mov_b32 m0, %0" \
                 : "=&s"(keep_) : "v"(voff), "s"(ldsaddr), "s"(rs), "s"(soff) : "memory"); } while (0)
#define WAIT_VR(n, r) asm volatile("s_waitcnt vmcnt(%1)" : "+v"(r) : "n"(n) : "memory")

#define STAMP0(i) do { } while (0)
#define STAMP(i) do { } while (0)
#define STAMP2(i) do { } while (0)
#define STAMPC(i) do { } while (0)
#define STAMP_ARG
#define STAMP_PASS

__device__ __forceinline__ void load_c(LAS unsigned char* smem, const u16* C, int lw, int lane, int rot) {
#if defined(__HIP_DEVICE_COMPILE__)
    const __amdgpu_buffer_rsrc_t rC = __builtin_amdgcn_make_buffer_rsrc((void*)C, 0, 80 * DM * 2, 0x00020000);
    int vo[3];
    #pragma unroll
    for (int q = 0; q < 3; ++q) {
        const int rg = q < 2 ? lw + 4 * q : 8 + (lw & 1);
        const int row = 8 * rg + (lane >> 3);
        vo[q] = perm80(row) * (DM * 2) + (((lane & 7) ^ ((row >> 1) & 7)) * 16);
    }
    const unsigned sb = (unsigned)(size_t)smem + L_C;
    #pragma unroll
    for (int k0 = 0; k0 < 10; ++k0) {
        int ks = k0 + rot; ks = ks >= 10 ? ks - 10 : ks;
        GLDS_ASM(rC, vo[0], ks * 128, sb + ks * 10240 + lw * 1024);
        GLDS_ASM(rC, vo[1], ks * 128, sb + ks * 10240 + (lw + 4) * 1024);
        if ((k0 & 1) == (lw >> 1)) GLDS_ASM(rC, vo[2], ks * 128, sb + ks * 10240 + (8 + (lw & 1)) * 1024);
    }
#endif
}
__device__ __forceinline__ void publish_x(LAS unsigned char* smem, char* ws, u64 gen, int ln, int tbS, int e2, int kq, int w, int lane,
                                          const u32x4& xa, const u32x4& xb, const u32x2& xc) {
#if defined(__HIP_DEVICE_COMPILE__)
    const int wm = w & 1, wn = w >> 1, fr = lane & 15, fq = lane >> 4;
    const __amdgpu_buffer_rsrc_t rX = __builtin_amdgcn_make_buffer_rsrc((void*)(ws + WS_XN + (size_t)ln * XN_BYTES + (size_t)tbS * 163840 + (4 * wm + kq) * 20480), 0, 20480, 0x00020000);
    const int cc0 = 20 * e2 + 10 * wn;
    ST16_SC1(rX, xa, (cc0 + fq) * 256 + fr * 16, 0);
    ST16_SC1(rX, xb, (cc0 + 4 + fq) * 256 + fr * 16, 0);
    ST8_SC1(rX, xc, (cc0 + 8 + (fq >> 1)) * 256 + fr * 16 + (fq & 1) * 8, 0);
    float v[20];
    #pragma unroll
    for (int q = 0; q < 4; ++q) { v[2 * q] = bf_lo(xa[q]); v[2 * q + 1] = bf_hi(xa[q]); v[8 + 2 * q] = bf_lo(xb[q]); v[9 + 2 * q] = bf_hi(xb[q]); }
    v[16] = bf_lo(xc[0]); v[17] = bf_hi(xc[0]); v[18] = bf_lo(xc[1]); v[19] = bf_hi(xc[1]);
    float s = 0.f;
    #pragma unroll
    for (int q = 0; q < 20; ++q) s += v[q];
    const float mean = sum_mod16(s) * (1.0f / 80.0f);
    float m2 = 0.f;
    #pragma unroll
    for (int q = 0; q < 20; ++q) { const float d = v[q] - mean; m2 += d * d; }
    m2 = sum_mod16(m2);
    const __amdgpu_buffer_rsrc_t rS = __builtin_amdgcn_make_buffer_rsrc((void*)(ws + WS_ST + ((size_t)(ln * 16 + tbS) * 8 + 2 * e2 + wn) * 1024), 0, 1024, 0x00020000);
    const u32x2 st = {__builtin_bit_cast(unsigned, mean), __builtin_bit_cast(unsigned, m2)};
    ST8_SC1(rS, st, lane < 16 ? (64 * wm + 16 * kq + lane) * 8 : 0x7ffffff0, 0);
    signal_last((LAS unsigned*)(smem + L_MISC + CNT_PUB), (unsigned)ln * 4 + 3, ws + WS_FX + ((size_t)(ln * 16 + tbS) * 16 + e2 * 4 + kq) * 128, gen + 1 + ln, lane);
#endif
}

}

__global__ __launch_bounds__(512, 2) void fnet_persistent(const u16* __restrict__ x_in,
        const u16* __restrict__ WreA, const u16* __restrict__ WimA, const u16* __restrict__ CreA, const u16* __restrict__ CimA,
        const u16* __restrict__ WreB, const u16* __restrict__ WimB, const u16* __restrict__ CreB, const u16* __restrict__ CimB,
        u16* __restrict__ out, char* __restrict__ ws) {
#if defined(__HIP_DEVICE_COMPILE__)
    extern __shared__ __attribute__((aligned(16))) unsigned char smem_[];
    LAS unsigned char* smem = (LAS unsigned char*)smem_;
    const int tid = threadIdx.x;
    int lane = tid & 63;
    const int w0 = __builtin_amdgcn_readfirstlane(tid >> 6);
    const int b = blockIdx.x;
#define GEOM(bb) const int xcd = (bb) & 7, idx = (bb) >> 3; \
    const int kq = xcd & 3, tbS = (xcd >> 2) * 8 + (idx >> 2), e2 = idx & 3; \
    const int tbH = (xcd >> 1) * 4 + (idx >> 3), j = (xcd & 1) * 8 + (idx & 7); \
    const int halfS = kq >> 1, halfH = j >> 3, eb = j & 7; (void)halfS; (void)halfH; (void)eb; (void)e2; (void)tbS;

    STAMP0(0);
    u64 gen = __hip_atomic_load((const GAS u64*)(ws + WS_GEN), __ATOMIC_RELAXED, __HIP_MEMORY_SCOPE_AGENT);
    gen = ((u64)(unsigned)__builtin_amdgcn_readfirstlane((unsigned)(gen >> 32)) << 32) | (unsigned)__builtin_amdgcn_readfirstlane((unsigned)gen);
    if (tid < 256) *(LAS unsigned*)(smem + L_MISC + tid * 4) = 0;
    WAIT_L(0);
    BAR();

    u32x4 xa = {0, 0, 0, 0}, xb = {0, 0, 0, 0}; u32x2 xc = {0, 0};
    {
        GEOM(b)
        const int w = w0;
        if (w >= 4) {
            __builtin_amdgcn_s_sleep(8);
            load_c(smem, (halfH ? CimA : CreA) + (size_t)eb * 80 * DM, w - 4, lane, ROT_C * (tbH & 3));
        } else {
            const u16* xp = x_in + (size_t)(tbS * 128 + 64 * (w & 1) + 16 * kq + (lane & 15)) * DM + 160 * e2 + 80 * (w >> 1);
            xa = *(const u32x4*)(xp + 8 * (lane >> 4)); xb = *(const u32x4*)(xp + 32 + 8 * (lane >> 4)); xc = *(const u32x2*)(xp + 64 + 4 * (lane >> 4));
            STAMP(1);
            publish_x(smem, ws, gen, 0, tbS, e2, kq, w, lane, xa, xb, xc);
        }
    }
    STAMP(2);

    for (int l = 0; l < 4; ++l) {
        const int set = l & 1;
        asm volatile("" : "+v"(lane));
        int bb = b; asm volatile("" : "+s"(bb));
        int w = w0; asm volatile("" : "+s"(w));
        GEOM(bb)
        STAMP(8 + 12 * l + 0);
        STAMPC(48 + 2 * l);
        {
            const int g = w ^ 4;
            const int fr = lane & 15, fq = lane >> 4;
            int rdC[2];
            #pragma unroll
            for (int kk = 0; kk < 2; ++kk) rdC[kk] = L_C + fr * 128 + (((4 * kk + fq) ^ ((fr >> 1) & 7)) * 16);
            const unsigned cc = (unsigned)(size_t)smem + L_MISC + CNT_C, cx = (unsigned)(size_t)smem + L_MISC + CNT_X, ccs = (unsigned)(size_t)smem + L_MISC + CNT_CS;
            if (w >= 4) {
                WAIT_V(0);
                lds_inc(cc);
                for (unsigned i2 = 0; lds_peek(cc) < 4u * (unsigned)(l + 1) && i2 < 400000000u; ++i2) __builtin_amdgcn_s_sleep(1);
                if (l < 2) {
                    const bf16x8 ones = {0x3f80, 0x3f80, 0x3f80, 0x3f80, 0x3f80, 0x3f80, 0x3f80, 0x3f80};
                    f32x4 cs[5];
                    #pragma unroll
                    for (int i = 0; i < 5; ++i) cs[i] = (f32x4){0.f, 0.f, 0.f, 0.f};
                    #pragma unroll
                    for (int q = 0; q < 5; ++q) {
                        const int ks = 4 * q + (w & 3);
                        #pragma unroll
                        for (int i = 0; i < 5; ++i)
                            cs[i] = __builtin_amdgcn_mfma_f32_16x16x32_bf16(ones, *(const LAS bf16x8*)(smem + ((ks & 1) ? rdC[1] : rdC[0]) + (ks >> 1) * 10240 + i * 2048), cs[i], 0, 0, 0);
                    }
                    if (fq == 0) {
                        #pragma unroll
                        for (int i = 0; i < 5; ++i) __hip_atomic_fetch_add((LAS float*)(smem + L_MISC + CSUM + set * 320) + 16 * i + fr, cs[i][0], __ATOMIC_RELAXED, __HIP_MEMORY_SCOPE_WORKGROUP);
                    }
                    WAIT_L(0);
                    lds_inc(ccs);
                }
                __builtin_amdgcn_s_setprio(2);
            }
            poll_n(ws + WS_FX + ((size_t)(l * 16 + tbH) * 16 + (lane & 3) * 4 + (g & 3)) * 128, 4, gen + 1 + l, lane, ws);
            const __amdgpu_buffer_rsrc_t rX = __builtin_amdgcn_make_buffer_rsrc((void*)(ws + WS_XN + (size_t)l * XN_BYTES + (size_t)tbH * 163840 + g * 20480), 0, 20480, 0x00020000);
            u32x4 xf[20];
            const int vo = lane * 16;
            const int rot = ROT_X * (j & 7);
            if (w < 4) for (unsigned i2 = 0; lds_peek(cx) < 4u * (unsigned)(l + 1) && i2 < 400000000u; ++i2) { }
            auto ldx = [&](int q) __attribute__((always_inline)) { int k2 = q + rot; k2 = k2 >= 10 ? k2 - 10 : k2; LD16x2_SC1(xf[2 * q], xf[2 * q + 1], rX, vo + k2 * 2048); };
            u32x2 sp0, sp1;
            { const __amdgpu_buffer_rsrc_t rS = __builtin_amdgcn_make_buffer_rsrc((void*)(ws + WS_ST + (size_t)(l * 16 + tbH) * 8192), 0, 8192, 0x00020000);
              const int so = (lane >> 4) * 1024 + (16 * g + (lane & 15)) * 8;
              asm volatile("s_nop 4\n\tbuffer_load_dwordx2 %0, %2, %4, 0 offen sc1\n\tbuffer_load_dwordx2 %1, %3, %4, 0 offen sc1"
                           : "=&v"(sp0), "=&v"(sp1) : "v"(so), "v"(so + 4096), "s"(rS) : "memory"); }
            #pragma unroll
            for (int q = 0; q < XPRE; ++q) ldx(q);
            STAMP2(1);
            f32x4 acc[5];
            #pragma unroll
            for (int i = 0; i < 5; ++i) acc[i] = (f32x4){0.f, 0.f, 0.f, 0.f};
            bf16x8 cf[CPRE + 1][5];
            auto rdc = [&](int ks) __attribute__((always_inline)) {
                int k2 = (ks >> 1) + rot; k2 = k2 >= 10 ? k2 - 10 : k2;
                #pragma unroll
                for (int i = 0; i < 5; ++i) cf[ks % (CPRE + 1)][i] = *(const LAS bf16x8*)(smem + rdC[ks & 1] + k2 * 10240 + i * 2048);
            };
            #pragma unroll
            for (int ks = 0; ks < CPRE; ++ks) rdc(ks);
            float mean = 0.f, rstd = 0.f, mr[4], rr[4];
            const float sg = halfH ? -1.0f : 1.0f;
            #pragma unroll
            for (int ks = 0; ks < 20; ++ks) {
                if ((ks & 1) == 0 && (ks >> 1) + XPRE < 10) ldx((ks >> 1) + XPRE);
                if (ks == XINC && w >= 4) lds_inc(cx);
                if (ks + CPRE < 20) rdc(ks + CPRE);
                WAIT_VR((2 * ((ks >> 1) + 1 + XPRE) < 20 ? 2 * ((ks >> 1) + 1 + XPRE) : 20) - ks - 1, xf[ks]);
                if (ks == 0) asm volatile("" : "+v"(sp0), "+v"(sp1));
                if (ks == 0) STAMP2(2);
                if (ks == 10) STAMP2(3);
                if (ks == 19) STAMP2(4);
                #pragma unroll
                for (int i = 0; i < 5; ++i) acc[i] = __builtin_amdgcn_mfma_f32_16x16x32_bf16(__builtin_bit_cast(bf16x8, xf[ks]), cf[ks % (CPRE + 1)][i], acc[i], 0, 0, 0);
                if (ks == 3) {
                    const unsigned a0 = sp0[0], a1 = sp0[1], b0 = sp1[0], b1 = sp1[1];
                    const float m0 = __builtin_bit_cast(float, a0), q0 = __builtin_bit_cast(float, a1), m1 = __builtin_bit_cast(float, b0), q1 = __builtin_bit_cast(float, b1);
                    mean = sum_mod16(m0 + m1) * 0.125f;
                    const float d0 = m0 - mean, d1 = m1 - mean;
                    rstd = 1.0f / sqrtf(sum_mod16((q0 + q1) + 80.0f * (d0 * d0 + d1 * d1)) * (1.0f / DM) + 1e-5f);
                    #pragma unroll
                    for (int r = 0; r < 4; ++r) {
                        mr[r] = __builtin_bit_cast(float, __builtin_amdgcn_ds_bpermute((4 * fq + r) * 4, __builtin_bit_cast(int, mean)));
                        rr[r] = sg * __builtin_bit_cast(float, __builtin_amdgcn_ds_bpermute((4 * fq + r) * 4, __builtin_bit_cast(int, rstd)));
                    }
                }
            }
            for (unsigned i2 = 0; lds_peek(ccs) < 4u * (unsigned)(l < 1 ? 1 : 2) && i2 < 400000000u; ++i2) { }
            STAMP2(5);
            unsigned d[5][2];
            #pragma unroll
            for (int i = 0; i < 5; ++i) {
                const float c = *((const LAS float*)(smem + L_MISC + CSUM + set * 320) + 16 * i + fr);
                d[i][0] = pk_bf16((acc[i][0] - mr[0] * c) * rr[0], (acc[i][1] - mr[1] * c) * rr[1]);
                d[i][1] = pk_bf16((acc[i][2] - mr[2] * c) * rr[2], (acc[i][3] - mr[3] * c) * rr[3]);
            }
            const int sigma = 2 * tbH + (g >> 2);
            const __amdgpu_buffer_rsrc_t rA = __builtin_amdgcn_make_buffer_rsrc((void*)(ws + WS_AB + (size_t)l * AB_BYTES + ((size_t)(halfH * 4 + (eb >> 1)) * 32 + sigma) * 20480 + (eb & 1) * 10240 + (g & 3) * 2560), 0, 2560, 0x00020000);
            #pragma unroll
            for (int p = 0; p < 2; ++p) {
                const auto r0 = __builtin_amdgcn_permlane16_swap(d[2 * p][0], d[2 * p + 1][0], false, false);
                const auto r1 = __builtin_amdgcn_permlane16_swap(d[2 * p][1], d[2 * p + 1][1], false, false);
                const u32x4 o = {r0[0], r1[0], r0[1], r1[1]};
                const int so = (16 * (fq & 1) + fr) * 32 + (fq >> 1) * 16;
                ST16_SC1(rA, o, so, p * 1024);
            }
            { const u32x2 o = {d[4][0], d[4][1]}; ST8_SC1(rA, o, fr * 32 + fq * 8, 2048); }
            STAMP2(6);
            if (w >= 4)
            signal_last((LAS unsigned*)(smem + L_MISC + (w >= 4 ? CNT_H0 : CNT_H1)), (unsigned)l * 4 + 3,
                        ws + WS_FH + (((size_t)(l * 16 + tbH) * 16 + j) * 2 + (g >> 2)) * 128, gen + 5 + l, lane);
            __builtin_amdgcn_s_setprio(0);
        }
        STAMP(8 + 12 * l + 1);
        STAMPC(49 + 2 * l);
        const char* hflag = ws + WS_FH + (((size_t)(l * 16 + 8 * (kq & 1) + ((lane & 15) >> 1)) * 16 + 8 * halfS + 2 * e2 + (lane & 1)) * 2) * 128;
        if (w >= 4) {
            const int lw = w - 4;
            const u16* W = set ? (halfS ? WimB : WreB) : (halfS ? WimA : WreA);
            const __amdgpu_buffer_rsrc_t rW = __builtin_amdgcn_make_buffer_rsrc((void*)(W + (size_t)tbS * 128 * SEQ + (kq & 1) * 1024), 0, 128 * SEQ * 2, 0x00020000);
            const __amdgpu_buffer_rsrc_t rA = __builtin_amdgcn_make_buffer_rsrc((void*)(ws + WS_AB + (size_t)l * AB_BYTES + ((size_t)(halfS * 4 + e2) * 32 + (kq & 1) * 16) * 20480), 0, 16 * 20480, 0x00020000);
            const int voffW0 = (lane >> 3) * (SEQ * 2) + (((lane & 7) ^ ((lane >> 4) & 7)) * 16);
            const int voffW1 = (lane >> 3) * (SEQ * 2) + (((lane & 7) ^ ((4 + (lane >> 4)) & 7)) * 16);
            const int voffW = (lw & 1) ? voffW1 : voffW0;
            const int voffA = lane * 16;
            auto sig = [&](int x) __attribute__((always_inline)) { return 2 * ((x + tbS + ROT_W * e2) & 7) + (x >> 3); };
            auto issue_w = [&](int x) __attribute__((always_inline)) {
                const int sg_ = sig(x), dst = w_slot(0) * ((x & 3) == 0) + w_slot(1) * ((x & 3) == 1) + w_slot(2) * ((x & 3) == 2) + w_slot(3) * ((x & 3) == 3);
                #pragma unroll
                for (int q = 0; q < 4; ++q) { const int i = lw + 4 * q;
                    __builtin_amdgcn_raw_ptr_buffer_load_lds(rW, (LAS void*)(smem + dst + i * 1024), 16, voffW, sg_ * 128 + i * 8 * (SEQ * 2), 0, W_AUX); }
            };
            auto issue_a = [&](int x) __attribute__((always_inline)) {
                const int sg_ = sig(x), dst = a_slot(0) * ((x & 3) == 0) + a_slot(1) * ((x & 3) == 1) + a_slot(2) * ((x & 3) == 2) + a_slot(3) * ((x & 3) == 3);
                #pragma unroll
                for (int q = 0; q < 5; ++q) { const int i = lw + 4 * q;
                    __builtin_amdgcn_raw_ptr_buffer_load_lds(rA, (LAS void*)(smem + dst + i * 1024), 16, voffA, sg_ * 20480 + i * 1024, 0, A_AUX); }
            };
            const unsigned go = (unsigned)(size_t)smem + L_MISC + CNT_GO;
            if (lw == 0) {
                poll_n(ws + WS_FH + (((size_t)(l * 16 + 8 * (kq & 1) + ((tbS + ROT_W * e2) & 7)) * 16 + 8 * halfS + 2 * e2 + (lane & 1)) * 2) * 128, 2, gen + 5 + l, lane, ws);
                lds_inc(go);
            } else {
                #pragma unroll
                for (int q = 0; q < 11; ++q) {
                    const int n = (lw - 1) + 3 * q;
                    if (n < 32) { const int x = n >> 4, i = n & 15;
                        __builtin_amdgcn_raw_ptr_buffer_load_lds(rW, (LAS void*)(smem + (x ? w_slot(1) : w_slot(0)) + i * 1024), 16, (i & 1) ? voffW1 : voffW0, sig(x) * 128 + i * 8 * (SEQ * 2), 0, W_AUX); }
                }
                for (unsigned i2 = 0; lds_peek(go) < (unsigned)(l + 1) && i2 < 40000000u; ++i2) __builtin_amdgcn_s_sleep(1);
            }
            issue_a(0);
            if (lw == 0) poll_n(hflag, 16, gen + 5 + l, lane, ws);
            WAIT_V(0);
            BAR();
            STAMP(8 + 12 * l + 3);
            issue_a(1); issue_w(2); issue_a(2); issue_w(3);
            WAIT_V(13); BAR(); issue_a(3);
            WAIT_V(9);  BAR(); issue_w(4); issue_a(4);
            for (int st = 2; st < 13; ++st) { WAIT_V(9); BAR(); issue_w(st + 3); issue_a(st + 3); }
            WAIT_V(9); BAR();
            __builtin_amdgcn_s_waitcnt(0x0F70); BAR();
            BAR();
            BAR();
            STAMP(8 + 12 * l + 4);
            if (l < 3) {
                const unsigned ce = (unsigned)(size_t)smem + L_MISC + CNT_E;
                for (unsigned i2 = 0; lds_peek(ce) < 4u * (unsigned)(l + 1) && i2 < 40000000u; ++i2) __builtin_amdgcn_s_sleep(1);
                load_c(smem, (set ? (halfH ? CimA : CreA) : (halfH ? CimB : CreB)) + (size_t)eb * 80 * DM, lw, lane, ROT_C * (tbH & 3));
            }
            STAMP2(14);
        } else {
            const int fr = lane & 15, fq = lane >> 4;
            const int wm = w & 1, wn = w >> 1;
            int rdT[2][4], rdE[2];
            #pragma unroll
            for (int kk = 0; kk < 2; ++kk) {
                #pragma unroll
                for (int jj = 0; jj < 4; ++jj) rdT[kk][jj] = (wm * 64 + 16 * ((jj + kq) & 3) + fr) * 128 + (((kk * 4 + fq) ^ ((fr >> 1) & 7)) * 16);
                rdE[kk] = wn * 10240 + (2 * kk + (fq >> 1)) * 2560 + fr * 32 + (fq & 1) * 16;
            }
            f32x4 acc[5][4];
            #pragma unroll
            for (int i = 0; i < 5; ++i)
                #pragma unroll
                for (int jj = 0; jj < 4; ++jj) acc[i][jj] = (f32x4){0.f, 0.f, 0.f, 0.f};
            bf16x8 ft[2][4], fe[2][5];
            auto rd = [&](int kk, int slot) __attribute__((always_inline)) {
                #pragma unroll
                for (int jj = 0; jj < 4; ++jj) ft[kk][jj] = *(const LAS bf16x8*)(smem + w_slot(slot) + rdT[kk][jj]);
                #pragma unroll
                for (int i = 0; i < 5; ++i) fe[kk][i] = *(const LAS bf16x8*)(smem + a_slot(slot) + rdE[kk] + i * 512);
            };
            auto mm = [&](int kk) __attribute__((always_inline)) {
                #pragma unroll
                for (int i = 0; i < 5; ++i)
                    #pragma unroll
                    for (int jj = 0; jj < 4; ++jj) acc[i][jj] = __builtin_amdgcn_mfma_f32_16x16x32_bf16(fe[kk][i], ft[kk][jj], acc[i][jj], 0, 0, 0);
                #pragma unroll
                for (int q = 0; q < 9; ++q) { __builtin_amdgcn_sched_group_barrier(0x8, 2, 0); __builtin_amdgcn_sched_group_barrier(0x100, 1, 0); }
                __builtin_amdgcn_sched_group_barrier(0x8, 2, 0);
            };
            BAR();
            STAMP(8 + 12 * l + 3);
            STAMPC(40 + 2 * l);
            rd(0, 0);
            for (int s0 = 0; s0 < 16; s0 += 4) {
                #pragma unroll
                for (int u = 0; u < 4; ++u) {
                    rd(1, u);
                    mm(0);
                    SCHED();
                    if (u == 0 && s0 == 0)
                        signal_last((LAS unsigned*)(smem + L_MISC + CNT_H1), (unsigned)l * 4 + 3, ws + WS_FH + (((size_t)(l * 16 + tbH) * 16 + j) * 2 + 1) * 128, gen + 5 + l, lane);
                    if (u == 1 && s0 == 4 && w == 0) poll_n(hflag + 128, 16, gen + 5 + l, lane, ws);
                    BAR();
                    SCHED();
                    rd(0, (u + 1) & 3);
                    mm(1);
                    SCHED();
                }
            }
            WAIT_L(0);
            BAR();
            STAMP(8 + 12 * l + 4);
            STAMPC(41 + 2 * l);
            char* pb = ws + WS_P + (size_t)l * P_BYTES + (size_t)(tbS * 4 + e2) * (64 * 2560);
            const __amdgpu_buffer_rsrc_t rP = __builtin_amdgcn_make_buffer_rsrc((void*)pb, 0, 64 * 2560, 0x00020000);
            #pragma unroll
            for (int jj = 1; jj < 4; ++jj) {
                const int po = ((((kq + jj) & 3) * 4 + kq) * 4 + w) * 2560;
                const u32x4 o0 = {pk_bf16(acc[0][jj][0], acc[0][jj][1]), pk_bf16(acc[0][jj][2], acc[0][jj][3]), pk_bf16(acc[1][jj][0], acc[1][jj][1]), pk_bf16(acc[1][jj][2], acc[1][jj][3])};
                ST16_SC1(rP, o0, po + lane * 16, 0);
                const u32x4 o1 = {pk_bf16(acc[2][jj][0], acc[2][jj][1]), pk_bf16(acc[2][jj][2], acc[2][jj][3]), pk_bf16(acc[3][jj][0], acc[3][jj][1]), pk_bf16(acc[3][jj][2], acc[3][jj][3])};
                ST16_SC1(rP, o1, po + 1024 + lane * 16, 0);
                const u32x2 o2 = {pk_bf16(acc[4][jj][0], acc[4][jj][1]), pk_bf16(acc[4][jj][2], acc[4][jj][3])};
                ST8_SC1(rP, o2, po + 2048 + lane * 8, 0);
            }
            STAMP2(12);
            signal_last((LAS unsigned*)(smem + L_MISC + CNT_E), (unsigned)l * 4 + 3, ws + WS_FS + ((size_t)(l * 16 + tbS) * 16 + e2 * 4 + kq) * 128, gen + 9 + l, lane);
            STAMP(8 + 12 * l + 5);
            poll_n(ws + WS_FS + ((size_t)(l * 16 + tbS) * 16 + e2 * 4 + ((kq + 1 + lane) & 3)) * 128, 3, gen + 9 + l, lane, ws);
            STAMP(8 + 12 * l + 9);
            u32x4 pa[3], pbv[3]; u32x2 pc[3];
            #pragma unroll
            for (int dq = 1; dq < 4; ++dq) {
                const int po = ((kq * 4 + ((kq + dq) & 3)) * 4 + w) * 2560;
                asm volatile("s_nop 4\n\tbuffer_load_dwordx4 %0, %3, %5, 0 offen sc1\n\tbuffer_load_dwordx4 %1, %3, %5, 0 offen offset:1024 sc1\n\tbuffer_load_dwordx2 %2, %4, %5, 0 offen offset:2048 sc1"
                             : "=&v"(pa[dq - 1]), "=&v"(pbv[dq - 1]), "=&v"(pc[dq - 1]) : "v"(po + lane * 16), "v"(po + lane * 8), "s"(rP) : "memory");
            }
            asm volatile("s_waitcnt vmcnt(0)" : "+v"(pa[0]), "+v"(pa[1]), "+v"(pa[2]), "+v"(pbv[0]), "+v"(pbv[1]), "+v"(pbv[2]), "+v"(pc[0]), "+v"(pc[1]), "+v"(pc[2]) :: "memory");
            STAMP2(21);
            #pragma unroll
            for (int q = 0; q < 4; ++q) {
                const float a0 = acc[q >> 1][0][2 * (q & 1)], a1 = acc[q >> 1][0][2 * (q & 1) + 1];
                xa[q] = pk_bf16(bf_lo(xa[q]) + ((a0 + bf_lo(pa[0][q])) + (bf_lo(pa[1][q]) + bf_lo(pa[2][q]))), bf_hi(xa[q]) + ((a1 + bf_hi(pa[0][q])) + (bf_hi(pa[1][q]) + bf_hi(pa[2][q]))));
                const float c0 = acc[2 + (q >> 1)][0][2 * (q & 1)], c1 = acc[2 + (q >> 1)][0][2 * (q & 1) + 1];
                xb[q] = pk_bf16(bf_lo(xb[q]) + ((c0 + bf_lo(pbv[0][q])) + (bf_lo(pbv[1][q]) + bf_lo(pbv[2][q]))), bf_hi(xb[q]) + ((c1 + bf_hi(pbv[0][q])) + (bf_hi(pbv[1][q]) + bf_hi(pbv[2][q]))));
            }
            #pragma unroll
            for (int q = 0; q < 2; ++q) {
                const float a0 = acc[4][0][2 * q], a1 = acc[4][0][2 * q + 1];
                xc[q] = pk_bf16(bf_lo(xc[q]) + ((a0 + bf_lo(pc[0][q])) + (bf_lo(pc[1][q]) + bf_lo(pc[2][q]))), bf_hi(xc[q]) + ((a1 + bf_hi(pc[0][q])) + (bf_hi(pc[1][q]) + bf_hi(pc[2][q]))));
            }
            if (l < 3) publish_x(smem, ws, gen, l + 1, tbS, e2, kq, w, lane, xa, xb, xc);
            else {
                u16* op = out + (size_t)(tbS * 128 + 64 * wm + 16 * kq + fr) * DM + 160 * e2 + 80 * wn;
                *(u32x4*)(op + 8 * fq) = xa; *(u32x4*)(op + 32 + 8 * fq) = xb; *(u32x2*)(op + 64 + 4 * fq) = xc;
            }
        }
        STAMP(8 + 12 * l + 10);
    }
    WAIT_V(0);
    if (b == 0 && tid == 0) __hip_atomic_store((GAS u64*)(ws + WS_GEN), gen + 16, __ATOMIC_RELAXED, __HIP_MEMORY_SCOPE_AGENT);
#endif
}

extern "C" void kernel_launch(void* const* d_in, const int* in_sizes, int n_in,
                              void* d_out, int out_size, void* d_ws, size_t ws_size,
                              hipStream_t stream) {
    (void)in_sizes; (void)n_in; (void)out_size; (void)ws_size;
    (void)hipFuncSetAttribute((const void*)fnet_persistent, hipFuncAttributeMaxDynamicSharedMemorySize, LDS_BYTES);
    fnet_persistent<<<256, 512, LDS_BYTES, stream>>>(
        (const u16*)d_in[0],
        (const u16*)d_in[1], (const u16*)d_in[2], (const u16*)d_in[3], (const u16*)d_in[4],
        (const u16*)d_in[6], (const u16*)d_in[7], (const u16*)d_in[8], (const u16*)d_in[9],
        (u16*)d_out, (char*)d_ws);
}
```

```cpp
#include <hip/hip_runtime.h>
#include <hip/hip_bf16.h>
#include <stdint.h>

typedef unsigned short u16;
typedef unsigned long long u64;
typedef short bf16x8 __attribute__((ext_vector_type(8)));
typedef float f32x4 __attribute__((ext_vector_type(4)));
typedef float f32x2 __attribute__((ext_vector_type(2)));
typedef unsigned u32x4 __attribute__((ext_vector_type(4)));
typedef unsigned u32x2 __attribute__((ext_vector_type(2)));
typedef __bf16 bf16x2v __attribute__((ext_vector_type(2)));
#define LAS __attribute__((address_space(3)))
#define GAS __attribute__((address_space(1)))

namespace {

constexpr int SEQ = 2048, DM = 640;

constexpr size_t WS_GEN = 0;
constexpr size_t WS_ERR = 256;
constexpr size_t WS_FX = 4096;
constexpr size_t WS_FH = WS_FX + 4 * 256 * 128;
constexpr size_t WS_FS = WS_FH + 4 * 512 * 128;
constexpr size_t WS_ST = 1u << 20;
constexpr size_t XN_BYTES = (size_t)SEQ * DM * 2;
constexpr size_t WS_XN = 2u << 20;
constexpr size_t WS_AB = WS_XN + 4 * XN_BYTES;
constexpr size_t AB_BYTES = 2 * XN_BYTES;
constexpr size_t WS_P = WS_AB + 4 * AB_BYTES;
constexpr size_t P_BYTES = 4 * XN_BYTES;
constexpr size_t WS_END = WS_P + 4 * P_BYTES;

constexpr int L_MISC = 0;
constexpr int L_C = 4096;
__device__ __forceinline__ constexpr int w_slot(int s) { return s == 0 ? 126976 : s == 1 ? 143360 : s == 2 ? 65536 : 81920; }
__device__ __forceinline__ constexpr int a_slot(int s) { return s == 0 ? 106496 : s == 1 ? 4096 : s == 2 ? 24576 : 45056; }
constexpr int LDS_BYTES = 163840;
constexpr int CNT_C = 0, CNT_H0 = 16, CNT_H1 = 32, CNT_GO = 48, CNT_PUB = 64, CNT_CS = 80, CNT_E = 96, CNT_X = 112, CSUM = 256;

#define WAIT_V(n) asm volatile("s_waitcnt vmcnt(%0)" ::"n"(n) : "memory")
#define WAIT_L(n) asm volatile("s_waitcnt lgkmcnt(%0)" ::"n"(n) : "memory")
#define BAR() do { asm volatile("" ::: "memory"); __builtin_amdgcn_s_barrier(); asm volatile("" ::: "memory"); } while (0)
#define SCHED() __builtin_amdgcn_sched_barrier(0)

__device__ __forceinline__ unsigned pk_bf16(float a, float b) {
    const f32x2 v = {a, b};
    return __builtin_bit_cast(unsigned, __builtin_convertvector(v, bf16x2v));
}
__device__ __forceinline__ float bf_lo(unsigned u) { return __builtin_bit_cast(float, u << 16); }
__device__ __forceinline__ float bf_hi(unsigned u) { return __builtin_bit_cast(float, u & 0xffff0000u); }

__device__ __forceinline__ int perm80(int r) {
    return r < 64 ? ((r >> 5) * 32 + ((r & 15) >> 2) * 8 + ((r >> 4) & 1) * 4 + (r & 3)) : r;
}

__device__ __forceinline__ float sum_mod16(float v) {
    { const unsigned u = __builtin_bit_cast(unsigned, v); const unsigned u2 = u;
      const auto r = __builtin_amdgcn_permlane16_swap(u, u2, false, false);
      const unsigned r0 = r[0], r1 = r[1]; v = __builtin_bit_cast(float, r0) + __builtin_bit_cast(float, r1); }
    { const unsigned u = __builtin_bit_cast(unsigned, v); const unsigned u2 = u;
      const auto r = __builtin_amdgcn_permlane32_swap(u, u2, false, false);
      const unsigned r0 = r[0], r1 = r[1]; v = __builtin_bit_cast(float, r0) + __builtin_bit_cast(float, r1); }
    return v;
}

constexpr unsigned SENT = 0xFFFFFFFFu;
__device__ __forceinline__ unsigned no_sent(unsigned u) { return u < 0xFFFFFFFEu ? u : 0xFFFFFFFEu; }
__device__ __forceinline__ unsigned umax(unsigned a, unsigned b) { return a > b ? a : b; }
__device__ __forceinline__ unsigned max4(const u32x4& v) { return umax(umax(v[0], v[1]), umax(v[2], v[3])); }
__device__ __forceinline__ unsigned lds_peek(unsigned addr) {
    unsigned v;
    asm volatile("ds_read_b32 %0, %1\n\ts_waitcnt lgkmcnt(0)" : "=&v"(v) : "v"(addr) : "memory");
    return __builtin_amdgcn_readfirstlane(v);
}
__device__ __forceinline__ void lds_inc(unsigned addr) {
    const unsigned one = 1;
    asm volatile("s_mov_b64 exec, 1\n\tds_add_u32 %0, %1\n\ts_mov_b64 exec, -1" :: "v"(addr), "v"(one) : "memory");
}
constexpr unsigned SPIN_CAP = 400000;
__device__ __forceinline__ bool spin_over(unsigned it, int lane, char* ws) {
    if ((it & 255u) != 255u) return false;
    GAS unsigned* err = (GAS unsigned*)(ws + WS_ERR);
    if (__builtin_amdgcn_readfirstlane(__hip_atomic_load(err, __ATOMIC_RELAXED, __HIP_MEMORY_SCOPE_AGENT)) == 0x600DBADu) return true;
    if (it > SPIN_CAP) { if (lane == 0) __hip_atomic_store(err, 0x600DBADu, __ATOMIC_RELAXED, __HIP_MEMORY_SCOPE_AGENT); return true; }
    return false;
}
#define W_AUX 0
#define A_AUX 16
#define HINT_SLEEP 2
#define CPRE 3
#define XPRE 3
#define XINC 4
#define ROT_W 1
#define ROT_W2 0
#define ROT_C 2
#define ROT_X 1

__device__ __forceinline__ void poll_n(const char* p  , int n, u64 tag, int lane, char* ws) {
    const GAS u64* f = (const GAS u64*)p;
    for (unsigned it = 0;; ++it) {
        u64 v = tag;
        if (lane < n) v = __hip_atomic_load(f, __ATOMIC_RELAXED, __HIP_MEMORY_SCOPE_AGENT);
        if (__all(v == tag) || spin_over(it, lane, ws)) break;
        __builtin_amdgcn_s_sleep(1);
    }
}

__device__ __forceinline__ void signal_last(LAS unsigned* cnt, unsigned last, char* flag, u64 tag, int lane) {
    asm volatile("s_waitcnt vmcnt(0)" ::: "memory");
    unsigned old = 0;
    if (lane == 0) old = __hip_atomic_fetch_add(cnt, 1u, __ATOMIC_RELAXED, __HIP_MEMORY_SCOPE_WORKGROUP);
    old = __builtin_amdgcn_readfirstlane(old);
    if (old == last && lane == 0) __hip_atomic_store((GAS u64*)flag, tag, __ATOMIC_RELAXED, __HIP_MEMORY_SCOPE_AGENT);
}

#define ST16_SC1(rs, v, voff, soff) __builtin_amdgcn_raw_buffer_store_b128((v), (rs), (voff) + (soff), 0, 16)
#define ST8_SC1(rs, v, voff, soff) __builtin_amdgcn_raw_buffer_store_b64((v), (rs), (voff) + (soff), 0, 16)
#define LD16x4_SC1(d0, d1, d2, d3, rs, voff) asm volatile("s_nop 4\n\tbuffer_load_dwordx4 %0, %4, %5, 0 offen sc1\n\tbuffer_load_dwordx4 %1, %4, %5, 0 offen offset:1024 sc1\n\t" \
    "buffer_load_dwordx4 %2, %4, %5, 0 offen offset:2048 sc1\n\tbuffer_load_dwordx4 %3, %4, %5, 0 offen offset:3072 sc1" \
    : "=&v"(d0), "=&v"(d1), "=&v"(d2), "=&v"(d3) : "v"(voff), "s"(rs) : "memory")
#define LD16x2_SC1(d0, d1, rs, voff) asm volatile("s_nop 4\n\tbuffer_load_dwordx4 %0, %2, %3, 0 offen sc1\n\tbuffer_load_dwordx4 %1, %2, %3, 0 offen offset:1024 sc1" \
    : "=&v"(d0), "=&v"(d1) : "v"(voff), "s"(rs) : "memory")
#define GLDS_ASM(rs, voff, soff, ldsaddr) do { unsigned keep_; \
    asm volatile("s_mov_b32 %0, m0\n\ts_mov_b32 m0, %2\n\ts_nop 4\n\tbuffer_load_dwordx4 %1, %3, %4 offen lds\n\ts_mov_b32 m0, %0" \
                 : "=&s"(keep_) : "v"(voff), "s"(ldsaddr), "s"(rs), "s"(soff) : "memory"); } while (0)
#define WAIT_VR(n, r) asm volatile("s_waitcnt vmcnt(%1)" : "+v"(r) : "n"(n) : "memory")

#define STAMP0(i) do { } while (0)
#define STAMP(i) do { } while (0)
#define STAMP2(i) do { } while (0)
#define STAMPC(i) do { } while (0)
#define STAMP_ARG
#define STAMP_PASS

__device__ __forceinline__ void load_c(LAS unsigned char* smem, const u16* C, int lw, int lane, int rot) {
#if defined(__HIP_DEVICE_COMPILE__)
    const __amdgpu_buffer_rsrc_t rC = __builtin_amdgcn_make_buffer_rsrc((void*)C, 0, 80 * DM * 2, 0x00020000);
    int vo[3];
    #pragma unroll
    for (int q = 0; q < 3; ++q) {
        const int rg = q < 2 ? lw + 4 * q : 8 + (lw & 1);
        const int row = 8 * rg + (lane >> 3);
        vo[q] = perm80(row) * (DM * 2) + (((lane & 7) ^ ((row >> 1) & 7)) * 16);
    }
    const unsigned sb = (unsigned)(size_t)smem + L_C;
    #pragma unroll
    for (int k0 = 0; k0 < 10; ++k0) {
        int ks = k0 + rot; ks = ks >= 10 ? ks - 10 : ks;
        GLDS_ASM(rC, vo[0], ks * 128, sb + ks * 10240 + lw * 1024);
        GLDS_ASM(rC, vo[1], ks * 128, sb + ks * 10240 + (lw + 4) * 1024);
        if ((k0 & 1) == (lw >> 1)) GLDS_ASM(rC, vo[2], ks * 128, sb + ks * 10240 + (8 + (lw & 1)) * 1024);
    }
#endif
}
template <bool FILL>
__device__ __forceinline__ void publish_x(char* ws, int ln, int tbS, int e2, int kq, int w, int lane, u32x4 xa, u32x4 xb, u32x2 xc) {
#if defined(__HIP_DEVICE_COMPILE__)
    const int wm = w & 1, wn = w >> 1, fr = lane & 15, fq = lane >> 4;
    const __amdgpu_buffer_rsrc_t rX = __builtin_amdgcn_make_buffer_rsrc((void*)(ws + WS_XN + (size_t)ln * XN_BYTES + (size_t)tbS * 163840 + (4 * wm + kq) * 20480), 0, 20480, 0x00020000);
    const __amdgpu_buffer_rsrc_t rS = __builtin_amdgcn_make_buffer_rsrc((void*)(ws + WS_ST + ((size_t)(ln * 16 + tbS) * 8 + 2 * e2 + wn) * 1024), 0, 1024, 0x00020000);
    const int cc0 = 20 * e2 + 10 * wn;
    u32x2 st = {SENT, SENT};
    if (FILL) { xa = (u32x4){SENT, SENT, SENT, SENT}; xb = xa; xc = st; }
    else {
        float v[20];
        #pragma unroll
        for (int q = 0; q < 4; ++q) { v[2 * q] = bf_lo(xa[q]); v[2 * q + 1] = bf_hi(xa[q]); v[8 + 2 * q] = bf_lo(xb[q]); v[9 + 2 * q] = bf_hi(xb[q]); xa[q] = no_sent(xa[q]); xb[q] = no_sent(xb[q]); }
        v[16] = bf_lo(xc[0]); v[17] = bf_hi(xc[0]); v[18] = bf_lo(xc[1]); v[19] = bf_hi(xc[1]);
        xc[0] = no_sent(xc[0]); xc[1] = no_sent(xc[1]);
        ST16_SC1(rX, xa, (cc0 + fq) * 256 + fr * 16, 0);
        ST16_SC1(rX, xb, (cc0 + 4 + fq) * 256 + fr * 16, 0);
        ST8_SC1(rX, xc, (cc0 + 8 + (fq >> 1)) * 256 + fr * 16 + (fq & 1) * 8, 0);
        float s = 0.f;
        #pragma unroll
        for (int q = 0; q < 20; ++q) s += v[q];
        const float mean = sum_mod16(s) * (1.0f / 80.0f);
        float m2 = 0.f;
        #pragma unroll
        for (int q = 0; q < 20; ++q) { const float d = v[q] - mean; m2 += d * d; }
        m2 = sum_mod16(m2);
        st = (u32x2){no_sent(__builtin_bit_cast(unsigned, mean)), no_sent(__builtin_bit_cast(unsigned, m2))};
    }
    if (FILL) {
        ST16_SC1(rX, xa, (cc0 + fq) * 256 + fr * 16, 0);
        ST16_SC1(rX, xb, (cc0 + 4 + fq) * 256 + fr * 16, 0);
        ST8_SC1(rX, xc, (cc0 + 8 + (fq >> 1)) * 256 + fr * 16 + (fq & 1) * 8, 0);
    }
    ST8_SC1(rS, st, lane < 16 ? (64 * wm + 16 * kq + lane) * 8 : 0x7ffffff0, 0);
#endif
}

}

__global__ __launch_bounds__(512, 2) void fnet_persistent(const u16* __restrict__ x_in,
        const u16* __restrict__ WreA, const u16* __restrict__ WimA, const u16* __restrict__ CreA, const u16* __restrict__ CimA,
        const u16* __restrict__ WreB, const u16* __restrict__ WimB, const u16* __restrict__ CreB, const u16* __restrict__ CimB,
        u16* __restrict__ out, char* __restrict__ ws) {
#if defined(__HIP_DEVICE_COMPILE__)
    extern __shared__ __attribute__((aligned(16))) unsigned char smem_[];
    LAS unsigned char* smem = (LAS unsigned char*)smem_;
    const int tid = threadIdx.x;
    int lane = tid & 63;
    const int w0 = __builtin_amdgcn_readfirstlane(tid >> 6);
    const int b = blockIdx.x;
#define GEOM(bb) const int xcd = (bb) & 7, idx = (bb) >> 3; \
    const int kq = xcd & 3, tbS = (xcd >> 2) * 8 + (idx >> 2), e2 = idx & 3; \
    const int tbH = (xcd >> 1) * 4 + (idx >> 3), j = (xcd & 1) * 8 + (idx & 7); \
    const int halfS = kq >> 1, halfH = j >> 3, eb = j & 7; (void)halfS; (void)halfH; (void)eb; (void)e2; (void)tbS;

    STAMP0(0);
    u32x4 xa = {0, 0, 0, 0}, xb = {0, 0, 0, 0}; u32x2 xc = {0, 0};
    u64 gen;
    {
        GEOM(b)
        const int w = w0;
        if (w < 4) {
            const u16* xp = x_in + (size_t)(tbS * 128 + 64 * (w & 1) + 16 * kq + (lane & 15)) * DM + 160 * e2 + 80 * (w >> 1);
            xa = *(const u32x4*)(xp + 8 * (lane >> 4)); xb = *(const u32x4*)(xp + 32 + 8 * (lane >> 4)); xc = *(const u32x2*)(xp + 64 + 4 * (lane >> 4));
        }
        u64 g0 = __hip_atomic_load((const GAS u64*)(ws + WS_GEN), __ATOMIC_RELAXED, __HIP_MEMORY_SCOPE_AGENT);
        if (tid < 256) *(LAS unsigned*)(smem + L_MISC + tid * 4) = 0;
        WAIT_L(0);
        BAR();
        if (w >= 4) load_c(smem, (halfH ? CimA : CreA) + (size_t)eb * 80 * DM, w - 4, lane, ROT_C * (tbH & 3));
        gen = ((u64)(unsigned)__builtin_amdgcn_readfirstlane((unsigned)(g0 >> 32)) << 32) | (unsigned)__builtin_amdgcn_readfirstlane((unsigned)g0);
        if (w < 4) {
            STAMP(1);
            publish_x<false>(ws, 0, tbS, e2, kq, w, lane, xa, xb, xc);
            signal_last((LAS unsigned*)(smem + L_MISC + CNT_PUB), 3u, ws + WS_FX + ((size_t)tbS * 16 + e2 * 4 + kq) * 128, gen + 1, lane);
            #pragma unroll
            for (int m = 1; m < 4; ++m) publish_x<true>(ws, m, tbS, e2, kq, w, lane, xa, xb, xc);
        }
    }
    STAMP(2);

    for (int l = 0; l < 4; ++l) {
        const int set = l & 1;
        asm volatile("" : "+v"(lane));
        int bb = b; asm volatile("" : "+s"(bb));
        int w = w0; asm volatile("" : "+s"(w));
        GEOM(bb)
        STAMP(8 + 12 * l + 0);
        STAMPC(48 + 2 * l);
        {
            const int g = w ^ 4;
            const int fr = lane & 15, fq = lane >> 4;
            int rdC[2];
            #pragma unroll
            for (int kk = 0; kk < 2; ++kk) rdC[kk] = L_C + fr * 128 + (((4 * kk + fq) ^ ((fr >> 1) & 7)) * 16);
            const unsigned cc = (unsigned)(size_t)smem + L_MISC + CNT_C, cx = (unsigned)(size_t)smem + L_MISC + CNT_X, ccs = (unsigned)(size_t)smem + L_MISC + CNT_CS;
            if (w >= 4) {
                WAIT_V(0);
                lds_inc(cc);
                for (unsigned i2 = 0; lds_peek(cc) < 4u * (unsigned)(l + 1) && i2 < 400000000u; ++i2) __builtin_amdgcn_s_sleep(1);
                if (l < 2) {
                    const bf16x8 ones = {0x3f80, 0x3f80, 0x3f80, 0x3f80, 0x3f80, 0x3f80, 0x3f80, 0x3f80};
                    f32x4 cs[5];
                    #pragma unroll
                    for (int i = 0; i < 5; ++i) cs[i] = (f32x4){0.f, 0.f, 0.f, 0.f};
                    #pragma unroll
                    for (int q = 0; q < 5; ++q) {
                        const int ks = 4 * q + (w & 3);
                        #pragma unroll
                        for (int i = 0; i < 5; ++i)
                            cs[i] = __builtin_amdgcn_mfma_f32_16x16x32_bf16(ones, *(const LAS bf16x8*)(smem + ((ks & 1) ? rdC[1] : rdC[0]) + (ks >> 1) * 10240 + i * 2048), cs[i], 0, 0, 0);
                    }
                    if (fq == 0) {
                        #pragma unroll
                        for (int i = 0; i < 5; ++i) __hip_atomic_fetch_add((LAS float*)(smem + L_MISC + CSUM + set * 320) + 16 * i + fr, cs[i][0], __ATOMIC_RELAXED, __HIP_MEMORY_SCOPE_WORKGROUP);
                    }
                    WAIT_L(0);
                    lds_inc(ccs);
                }
                __builtin_amdgcn_s_setprio(2);
            }
            if (l == 0) poll_n(ws + WS_FX + ((size_t)tbH * 16 + (lane & 3) * 4 + (g & 3)) * 128, 4, gen + 1, lane, ws);
            const __amdgpu_buffer_rsrc_t rX = __builtin_amdgcn_make_buffer_rsrc((void*)(ws + WS_XN + (size_t)l * XN_BYTES + (size_t)tbH * 163840 + g * 20480), 0, 20480, 0x00020000);
            const __amdgpu_buffer_rsrc_t rS = __builtin_amdgcn_make_buffer_rsrc((void*)(ws + WS_ST + (size_t)(l * 16 + tbH) * 8192), 0, 8192, 0x00020000);
            u32x4 xf[20];
            const int vo = lane * 16;
            const int rot = ROT_X * (j & 7);
            if (w < 4) for (unsigned i2 = 0; lds_peek(cx) < 4u * (unsigned)(l + 1) && i2 < 400000000u; ++i2) { }
            auto ldx = [&](int q) __attribute__((always_inline)) { int k2 = q + rot; k2 = k2 >= 10 ? k2 - 10 : k2; LD16x2_SC1(xf[2 * q], xf[2 * q + 1], rX, vo + k2 * 2048); };
            f32x4 acc[5];
            float mr[4], rr[4];
            const float sg = halfH ? -1.0f : 1.0f;
            bool first = true;
            for (unsigned it = 0;; ++it) {
                u32x2 sp0, sp1;
                { const int so = (lane >> 4) * 1024 + (16 * g + (lane & 15)) * 8;
                  asm volatile("s_nop 4\n\tbuffer_load_dwordx2 %0, %2, %4, 0 offen sc1\n\tbuffer_load_dwordx2 %1, %3, %4, 0 offen sc1"
                               : "=&v"(sp0), "=&v"(sp1) : "v"(so), "v"(so + 4096), "s"(rS) : "memory"); }
                asm volatile("s_waitcnt vmcnt(0)" : "+v"(sp0), "+v"(sp1) :: "memory");
                unsigned m = umax(umax(sp0[0], sp0[1]), umax(sp1[0], sp1[1]));
                if (__any(m == SENT) && !spin_over(it, lane, ws)) { __builtin_amdgcn_s_sleep(HINT_SLEEP); continue; }
                #pragma unroll
                for (int q = 0; q < XPRE; ++q) ldx(q);
                STAMP2(1);
                #pragma unroll
                for (int i = 0; i < 5; ++i) acc[i] = (f32x4){0.f, 0.f, 0.f, 0.f};
                bf16x8 cf[CPRE + 1][5];
                auto rdc = [&](int ks) __attribute__((always_inline)) {
                    int k2 = (ks >> 1) + rot; k2 = k2 >= 10 ? k2 - 10 : k2;
                    #pragma unroll
                    for (int i = 0; i < 5; ++i) cf[ks % (CPRE + 1)][i] = *(const LAS bf16x8*)(smem + rdC[ks & 1] + k2 * 10240 + i * 2048);
                };
                #pragma unroll
                for (int ks = 0; ks < CPRE; ++ks) rdc(ks);
                #pragma unroll
                for (int ks = 0; ks < 20; ++ks) {
                    if ((ks & 1) == 0 && (ks >> 1) + XPRE < 10) ldx((ks >> 1) + XPRE);
                    if (ks == XINC && w >= 4 && first) lds_inc(cx);
                    if (ks + CPRE < 20) rdc(ks + CPRE);
                    { WAIT_VR((2 * ((ks >> 1) + 1 + XPRE) < 20 ? 2 * ((ks >> 1) + 1 + XPRE) : 20) - ks - 1, xf[ks]); m = umax(m, max4(xf[ks])); }
                    if (ks == 0) STAMP2(2);
                    if (ks == 10) STAMP2(3);
                    if (ks == 19) STAMP2(4);
                    #pragma unroll
                    for (int i = 0; i < 5; ++i) acc[i] = __builtin_amdgcn_mfma_f32_16x16x32_bf16(__builtin_bit_cast(bf16x8, xf[ks]), cf[ks % (CPRE + 1)][i], acc[i], 0, 0, 0);
                    if (ks == 3) {
                        const unsigned a0 = sp0[0], a1 = sp0[1], b0 = sp1[0], b1 = sp1[1];
                        const float m0 = __builtin_bit_cast(float, a0), q0 = __builtin_bit_cast(float, a1), m1 = __builtin_bit_cast(float, b0), q1 = __builtin_bit_cast(float, b1);
                        const float mean = sum_mod16(m0 + m1) * 0.125f;
                        const float d0 = m0 - mean, d1 = m1 - mean;
                        const float rstd = 1.0f / sqrtf(sum_mod16((q0 + q1) + 80.0f * (d0 * d0 + d1 * d1)) * (1.0f / DM) + 1e-5f);
                        #pragma unroll
                        for (int r = 0; r < 4; ++r) {
                            mr[r] = __builtin_bit_cast(float, __builtin_amdgcn_ds_bpermute((4 * fq + r) * 4, __builtin_bit_cast(int, mean)));
                            rr[r] = sg * __builtin_bit_cast(float, __builtin_amdgcn_ds_bpermute((4 * fq + r) * 4, __builtin_bit_cast(int, rstd)));
                        }
                    }
                }
                first = false;
                if (!__any(m == SENT) || spin_over(it | 255u, lane, ws)) break;
            }
            for (unsigned i2 = 0; lds_peek(ccs) < 4u * (unsigned)(l < 1 ? 1 : 2) && i2 < 400000000u; ++i2) { }
            STAMP2(5);
            unsigned d[5][2];
            #pragma unroll
            for (int i = 0; i < 5; ++i) {
                const float c = *((const LAS float*)(smem + L_MISC + CSUM + set * 320) + 16 * i + fr);
                d[i][0] = pk_bf16((acc[i][0] - mr[0] * c) * rr[0], (acc[i][1] - mr[1] * c) * rr[1]);
                d[i][1] = pk_bf16((acc[i][2] - mr[2] * c) * rr[2], (acc[i][3] - mr[3] * c) * rr[3]);
            }
            const int sigma = 2 * tbH + (g >> 2);
            const __amdgpu_buffer_rsrc_t rA = __builtin_amdgcn_make_buffer_rsrc((void*)(ws + WS_AB + (size_t)l * AB_BYTES + ((size_t)(halfH * 4 + (eb >> 1)) * 32 + sigma) * 20480 + (eb & 1) * 10240 + (g & 3) * 2560), 0, 2560, 0x00020000);
            #pragma unroll
            for (int p = 0; p < 2; ++p) {
                const auto r0 = __builtin_amdgcn_permlane16_swap(d[2 * p][0], d[2 * p + 1][0], false, false);
                const auto r1 = __builtin_amdgcn_permlane16_swap(d[2 * p][1], d[2 * p + 1][1], false, false);
                const u32x4 o = {r0[0], r1[0], r0[1], r1[1]};
                const int so = (16 * (fq & 1) + fr) * 32 + (fq >> 1) * 16;
                ST16_SC1(rA, o, so, p * 1024);
            }
            { const u32x2 o = {d[4][0], d[4][1]}; ST8_SC1(rA, o, fr * 32 + fq * 8, 2048); }
            STAMP2(6);
            if (w >= 4)
            signal_last((LAS unsigned*)(smem + L_MISC + (w >= 4 ? CNT_H0 : CNT_H1)), (unsigned)l * 4 + 3,
                        ws + WS_FH + (((size_t)(l * 16 + tbH) * 16 + j) * 2 + (g >> 2)) * 128, gen + 5 + l, lane);
            __builtin_amdgcn_s_setprio(0);
        }
        STAMP(8 + 12 * l + 1);
        STAMPC(49 + 2 * l);
        const int rotw = l < 2 ? ROT_W : ROT_W2;
        const char* hflag = ws + WS_FH + (((size_t)(l * 16 + 8 * (kq & 1) + ((lane & 15) >> 1)) * 16 + 8 * halfS + 2 * e2 + (lane & 1)) * 2) * 128;
        if (w >= 4) {
            const int lw = w - 4;
            const u16* W = set ? (halfS ? WimB : WreB) : (halfS ? WimA : WreA);
            const __amdgpu_buffer_rsrc_t rW = __builtin_amdgcn_make_buffer_rsrc((void*)(W + (size_t)tbS * 128 * SEQ + (kq & 1) * 1024), 0, 128 * SEQ * 2, 0x00020000);
            const __amdgpu_buffer_rsrc_t rA = __builtin_amdgcn_make_buffer_rsrc((void*)(ws + WS_AB + (size_t)l * AB_BYTES + ((size_t)(halfS * 4 + e2) * 32 + (kq & 1) * 16) * 20480), 0, 16 * 20480, 0x00020000);
            const int voffW0 = (lane >> 3) * (SEQ * 2) + (((lane & 7) ^ ((lane >> 4) & 7)) * 16);
            const int voffW1 = (lane >> 3) * (SEQ * 2) + (((lane & 7) ^ ((4 + (lane >> 4)) & 7)) * 16);
            const int voffW = (lw & 1) ? voffW1 : voffW0;
            const int voffA = lane * 16;
            auto sig = [&](int x) __attribute__((always_inline)) { return 2 * ((x + tbS + rotw * e2) & 7) + (x >> 3); };
            auto wrow = [&](int i) __attribute__((always_inline)) { return 64 * (i >> 3) + 16 * (((i >> 1) + kq) & 3) + 8 * (i & 1); };
            auto issue_w = [&](int x) __attribute__((always_inline)) {
                const int sg_ = sig(x), dst = w_slot(0) * ((x & 3) == 0) + w_slot(1) * ((x & 3) == 1) + w_slot(2) * ((x & 3) == 2) + w_slot(3) * ((x & 3) == 3);
                #pragma unroll
                for (int q = 0; q < 4; ++q) { const int i = lw + 4 * q;
                    __builtin_amdgcn_raw_ptr_buffer_load_lds(rW, (LAS void*)(smem + dst + i * 1024), 16, voffW, sg_ * 128 + wrow(i) * (SEQ * 2), 0, W_AUX); }
            };
            auto issue_a = [&](int x) __attribute__((always_inline)) {
                const int sg_ = sig(x), dst = a_slot(0) * ((x & 3) == 0) + a_slot(1) * ((x & 3) == 1) + a_slot(2) * ((x & 3) == 2) + a_slot(3) * ((x & 3) == 3);
                #pragma unroll
                for (int q = 0; q < 5; ++q) { const int i = lw + 4 * q;
                    __builtin_amdgcn_raw_ptr_buffer_load_lds(rA, (LAS void*)(smem + dst + i * 1024), 16, voffA, sg_ * 20480 + i * 1024, 0, A_AUX); }
            };
            const unsigned go = (unsigned)(size_t)smem + L_MISC + CNT_GO;
            if (lw == 0) {
                poll_n(ws + WS_FH + (((size_t)(l * 16 + 8 * (kq & 1) + ((tbS + rotw * e2) & 7)) * 16 + 8 * halfS + 2 * e2 + (lane & 1)) * 2) * 128, 2, gen + 5 + l, lane, ws);
                lds_inc(go);
            } else {
                #pragma unroll
                for (int q = 0; q < 11; ++q) {
                    const int n = (lw - 1) + 3 * q;
                    if (n < 32) { const int x = n >> 4, i = n & 15;
                        __builtin_amdgcn_raw_ptr_buffer_load_lds(rW, (LAS void*)(smem + (x ? w_slot(1) : w_slot(0)) + i * 1024), 16, (i & 1) ? voffW1 : voffW0, sig(x) * 128 + wrow(i) * (SEQ * 2), 0, W_AUX); }
                }
                for (unsigned i2 = 0; lds_peek(go) < (unsigned)(l + 1) && i2 < 40000000u; ++i2) __builtin_amdgcn_s_sleep(1);
            }
            issue_a(0);
            if (lw == 0) poll_n(hflag, 16, gen + 5 + l, lane, ws);
            WAIT_V(0);
            BAR();
            STAMP(8 + 12 * l + 3);
            issue_a(1); issue_w(2); issue_a(2); issue_w(3);
            WAIT_V(13); BAR(); issue_a(3);
            WAIT_V(9);  BAR(); issue_w(4); issue_a(4);
            for (int st = 2; st < 13; ++st) { WAIT_V(9); BAR(); issue_w(st + 3); issue_a(st + 3); }
            WAIT_V(9); BAR();
            __builtin_amdgcn_s_waitcnt(0x0F70); BAR();
            BAR();
            BAR();
            STAMP(8 + 12 * l + 4);
            if (l < 3) {
                const unsigned ce = (unsigned)(size_t)smem + L_MISC + CNT_E;
                for (unsigned i2 = 0; lds_peek(ce) < 4u * (unsigned)(l + 1) && i2 < 40000000u; ++i2) __builtin_amdgcn_s_sleep(1);
                load_c(smem, (set ? (halfH ? CimA : CreA) : (halfH ? CimB : CreB)) + (size_t)eb * 80 * DM, lw, lane, ROT_C * (tbH & 3));
            }
            STAMP2(14);
        } else {
            const int fr = lane & 15, fq = lane >> 4;
            const int wm = w & 1, wn = w >> 1;
            int rdT[2], rdE[2];
            #pragma unroll
            for (int kk = 0; kk < 2; ++kk) {
                rdT[kk] = (wm * 64 + fr) * 128 + (((kk * 4 + fq) ^ ((fr >> 1) & 7)) * 16);
                rdE[kk] = wn * 10240 + (2 * kk + (fq >> 1)) * 2560 + fr * 32 + (fq & 1) * 16;
            }
            f32x4 acc[5][4];
            #pragma unroll
            for (int i = 0; i < 5; ++i)
                #pragma unroll
                for (int jj = 0; jj < 4; ++jj) acc[i][jj] = (f32x4){0.f, 0.f, 0.f, 0.f};
            bf16x8 ft[2][4], fe[2][5];
            auto rd = [&](int kk, int slot) __attribute__((always_inline)) {
                #pragma unroll
                for (int jj = 0; jj < 4; ++jj) ft[kk][jj] = *(const LAS bf16x8*)(smem + w_slot(slot) + rdT[kk] + jj * 2048);
                #pragma unroll
                for (int i = 0; i < 5; ++i) fe[kk][i] = *(const LAS bf16x8*)(smem + a_slot(slot) + rdE[kk] + i * 512);
            };
            auto mm = [&](int kk) __attribute__((always_inline)) {
                #pragma unroll
                for (int i = 0; i < 5; ++i)
                    #pragma unroll
                    for (int jj = 0; jj < 4; ++jj) acc[i][jj] = __builtin_amdgcn_mfma_f32_16x16x32_bf16(fe[kk][i], ft[kk][jj], acc[i][jj], 0, 0, 0);
                #pragma unroll
                for (int q = 0; q < 9; ++q) { __builtin_amdgcn_sched_group_barrier(0x8, 2, 0); __builtin_amdgcn_sched_group_barrier(0x100, 1, 0); }
                __builtin_amdgcn_sched_group_barrier(0x8, 2, 0);
            };
            BAR();
            STAMP(8 + 12 * l + 3);
            STAMPC(40 + 2 * l);
            rd(0, 0);
            for (int s0 = 0; s0 < 16; s0 += 4) {
                #pragma unroll
                for (int u = 0; u < 4; ++u) {
                    rd(1, u);
                    mm(0);
                    SCHED();
                    if (u == 0 && s0 == 0)
                        signal_last((LAS unsigned*)(smem + L_MISC + CNT_H1), (unsigned)l * 4 + 3, ws + WS_FH + (((size_t)(l * 16 + tbH) * 16 + j) * 2 + 1) * 128, gen + 5 + l, lane);
                    if (l == 0 && u == 1 && s0 == 8 && w == 0) {
                        const int xe = (lane >> 2) & 3, xk = lane & 3;
                        const int bx = (xk + 4 * (tbH >> 3)) + 8 * ((tbH & 7) * 4 + xe), xx = bx & 7, xi = bx >> 3;
                        poll_n(ws + WS_FH + (((size_t)((xx >> 1) * 4 + (xi >> 3)) * 16 + (xx & 1) * 8 + (xi & 7)) * 2 + 1) * 128, 16, gen + 5, lane, ws);
                    }
                    if (u == 1 && s0 == 4 && w == 0) poll_n(hflag + 128, 16, gen + 5 + l, lane, ws);
                    BAR();
                    SCHED();
                    rd(0, (u + 1) & 3);
                    mm(1);
                    SCHED();
                }
            }
            WAIT_L(0);
            BAR();
            STAMP(8 + 12 * l + 4);
            STAMPC(41 + 2 * l);
            char* pb = ws + WS_P + (size_t)l * P_BYTES + (size_t)(tbS * 4 + e2) * (64 * 2560);
            const __amdgpu_buffer_rsrc_t rP = __builtin_amdgcn_make_buffer_rsrc((void*)pb, 0, 64 * 2560, 0x00020000);
            #pragma unroll
            for (int jj = 1; jj < 4; ++jj) {
                const int po = ((((kq + jj) & 3) * 4 + kq) * 4 + w) * 2560;
                const u32x4 o0 = {pk_bf16(acc[0][jj][0], acc[0][jj][1]), pk_bf16(acc[0][jj][2], acc[0][jj][3]), pk_bf16(acc[1][jj][0], acc[1][jj][1]), pk_bf16(acc[1][jj][2], acc[1][jj][3])};
                ST16_SC1(rP, o0, po + lane * 16, 0);
                const u32x4 o1 = {pk_bf16(acc[2][jj][0], acc[2][jj][1]), pk_bf16(acc[2][jj][2], acc[2][jj][3]), pk_bf16(acc[3][jj][0], acc[3][jj][1]), pk_bf16(acc[3][jj][2], acc[3][jj][3])};
                ST16_SC1(rP, o1, po + 1024 + lane * 16, 0);
                const u32x2 o2 = {pk_bf16(acc[4][jj][0], acc[4][jj][1]), pk_bf16(acc[4][jj][2], acc[4][jj][3])};
                ST8_SC1(rP, o2, po + 2048 + lane * 8, 0);
            }
            STAMP2(12);
            signal_last((LAS unsigned*)(smem + L_MISC + CNT_E), (unsigned)l * 4 + 3, ws + WS_FS + ((size_t)(l * 16 + tbS) * 16 + e2 * 4 + kq) * 128, gen + 9 + l, lane);
            STAMP(8 + 12 * l + 5);
            poll_n(ws + WS_FS + ((size_t)(l * 16 + tbS) * 16 + e2 * 4 + ((kq + 1 + lane) & 3)) * 128, 3, gen + 9 + l, lane, ws);
            STAMP(8 + 12 * l + 9);
            u32x4 pa[3], pbv[3]; u32x2 pc[3];
            #pragma unroll
            for (int dq = 1; dq < 4; ++dq) {
                const int po = ((kq * 4 + ((kq + dq) & 3)) * 4 + w) * 2560;
                asm volatile("s_nop 4\n\tbuffer_load_dwordx4 %0, %3, %5, 0 offen sc1\n\tbuffer_load_dwordx4 %1, %3, %5, 0 offen offset:1024 sc1\n\tbuffer_load_dwordx2 %2, %4, %5, 0 offen offset:2048 sc1"
                             : "=&v"(pa[dq - 1]), "=&v"(pbv[dq - 1]), "=&v"(pc[dq - 1]) : "v"(po + lane * 16), "v"(po + lane * 8), "s"(rP) : "memory");
            }
            asm volatile("s_waitcnt vmcnt(0)" : "+v"(pa[0]), "+v"(pa[1]), "+v"(pa[2]), "+v"(pbv[0]), "+v"(pbv[1]), "+v"(pbv[2]), "+v"(pc[0]), "+v"(pc[1]), "+v"(pc[2]) :: "memory");
            STAMP2(21);
            #pragma unroll
            for (int q = 0; q < 4; ++q) {
                const float a0 = acc[q >> 1][0][2 * (q & 1)], a1 = acc[q >> 1][0][2 * (q & 1) + 1];
                xa[q] = pk_bf16(bf_lo(xa[q]) + ((a0 + bf_lo(pa[0][q])) + (bf_lo(pa[1][q]) + bf_lo(pa[2][q]))), bf_hi(xa[q]) + ((a1 + bf_hi(pa[0][q])) + (bf_hi(pa[1][q]) + bf_hi(pa[2][q]))));
                const float c0 = acc[2 + (q >> 1)][0][2 * (q & 1)], c1 = acc[2 + (q >> 1)][0][2 * (q & 1) + 1];
                xb[q] = pk_bf16(bf_lo(xb[q]) + ((c0 + bf_lo(pbv[0][q])) + (bf_lo(pbv[1][q]) + bf_lo(pbv[2][q]))), bf_hi(xb[q]) + ((c1 + bf_hi(pbv[0][q])) + (bf_hi(pbv[1][q]) + bf_hi(pbv[2][q]))));
            }
            #pragma unroll
            for (int q = 0; q < 2; ++q) {
                const float a0 = acc[4][0][2 * q], a1 = acc[4][0][2 * q + 1];
                xc[q] = pk_bf16(bf_lo(xc[q]) + ((a0 + bf_lo(pc[0][q])) + (bf_lo(pc[1][q]) + bf_lo(pc[2][q]))), bf_hi(xc[q]) + ((a1 + bf_hi(pc[0][q])) + (bf_hi(pc[1][q]) + bf_hi(pc[2][q]))));
            }
            if (l < 3) publish_x<false>(ws, l + 1, tbS, e2, kq, w, lane, xa, xb, xc);
            else {
                const __amdgpu_buffer_rsrc_t rO = __builtin_amdgcn_make_buffer_rsrc((void*)(out + (size_t)(tbS * 128 + 64 * wm + 16 * kq) * DM + 160 * e2 + 80 * wn), 0, 16 * DM * 2, 0x00020000);
                ST16_SC1(rO, xa, fr * (DM * 2) + 16 * fq, 0); ST16_SC1(rO, xb, fr * (DM * 2) + 64 + 16 * fq, 0); ST8_SC1(rO, xc, fr * (DM * 2) + 128 + 8 * fq, 0);
            }
        }
        STAMP(8 + 12 * l + 10);
    }
    WAIT_V(0);
    if (b == 0 && tid == 0) __hip_atomic_store((GAS u64*)(ws + WS_GEN), gen + 16, __ATOMIC_RELAXED, __HIP_MEMORY_SCOPE_AGENT);
#endif
}

extern "C" void kernel_launch(void* const* d_in, const int* in_sizes, int n_in,
                              void* d_out, int out_size, void* d_ws, size_t ws_size,
                              hipStream_t stream) {
    (void)in_sizes; (void)n_in; (void)out_size; (void)ws_size;
    (void)hipFuncSetAttribute((const void*)fnet_persistent, hipFuncAttributeMaxDynamicSharedMemorySize, LDS_BYTES);
    fnet_persistent<<<256, 512, LDS_BYTES, stream>>>(
        (const u16*)d_in[0],
        (const u16*)d_in[1], (const u16*)d_in[2], (const u16*)d_in[3], (const u16*)d_in[4],
        (const u16*)d_in[6], (const u16*)d_in[7], (const u16*)d_in[8], (const u16*)d_in[9],
        (u16*)d_out, (char*)d_ws);
}
```

```cpp
#define NO_TAIL 1
#define CODEWARM 20480
#include <hip/hip_runtime.h>
#include <hip/hip_bf16.h>
#include <stdint.h>

typedef unsigned short u16;
typedef unsigned long long u64;
typedef short bf16x8 __attribute__((ext_vector_type(8)));
typedef float f32x4 __attribute__((ext_vector_type(4)));
typedef float f32x2 __attribute__((ext_vector_type(2)));
typedef unsigned u32x4 __attribute__((ext_vector_type(4)));
typedef unsigned u32x2 __attribute__((ext_vector_type(2)));
typedef __bf16 bf16x2v __attribute__((ext_vector_type(2)));
#define LAS __attribute__((address_space(3)))
#define GAS __attribute__((address_space(1)))

namespace {

constexpr int SEQ = 2048, DM = 640;

constexpr size_t WS_GEN = 0;
constexpr size_t WS_ERR = 256;
constexpr size_t WS_FX = 4096;
constexpr size_t WS_FH = WS_FX + 4 * 256 * 128;
constexpr size_t WS_FS = WS_FH + 4 * 512 * 128;
constexpr size_t WS_ST = 1u << 20;
constexpr size_t XN_BYTES = (size_t)SEQ * DM * 2;
constexpr size_t WS_XN = 2u << 20;
constexpr size_t WS_AB = WS_XN + 4 * XN_BYTES;
constexpr size_t AB_BYTES = 2 * XN_BYTES;
constexpr size_t WS_P = WS_AB + 4 * AB_BYTES;
constexpr size_t P_BYTES = 4 * XN_BYTES;
constexpr size_t WS_END = WS_P + 4 * P_BYTES;

constexpr int L_MISC = 0;
constexpr int L_C = 4096;
__device__ __forceinline__ constexpr int w_slot(int s) { return s == 0 ? 126976 : s == 1 ? 143360 : s == 2 ? 65536 : 81920; }
__device__ __forceinline__ constexpr int a_slot(int s) { return s == 0 ? 106496 : s == 1 ? 4096 : s == 2 ? 24576 : 45056; }
constexpr int LDS_BYTES = 163840;
constexpr int CNT_C = 0, CNT_H0 = 16, CNT_H1 = 32, CNT_GO = 48, CNT_PUB = 64, CNT_CS = 80, CNT_E = 96, CNT_X = 112, CSUM = 256;

#define WAIT_V(n) asm volatile("s_waitcnt vmcnt(%0)" ::"n"(n) : "memory")
#define WAIT_L(n) asm volatile("s_waitcnt lgkmcnt(%0)" ::"n"(n) : "memory")
#define BAR() do { asm volatile("" ::: "memory"); __builtin_amdgcn_s_barrier(); asm volatile("" ::: "memory"); } while (0)
#define SCHED() __builtin_amdgcn_sched_barrier(0)

__device__ __forceinline__ unsigned pk_bf16(float a, float b) {
    const f32x2 v = {a, b};
    return __builtin_bit_cast(unsigned, __builtin_convertvector(v, bf16x2v));
}
__device__ __forceinline__ float bf_lo(unsigned u) { return __builtin_bit_cast(float, u << 16); }
__device__ __forceinline__ float bf_hi(unsigned u) { return __builtin_bit_cast(float, u & 0xffff0000u); }

__device__ __forceinline__ int perm80(int r) {
    return r < 64 ? ((r >> 5) * 32 + ((r & 15) >> 2) * 8 + ((r >> 4) & 1) * 4 + (r & 3)) : r;
}

__device__ __forceinline__ float sum_mod16(float v) {
    { const unsigned u = __builtin_bit_cast(unsigned, v); const unsigned u2 = u;
      const auto r = __builtin_amdgcn_permlane16_swap(u, u2, false, false);
      const unsigned r0 = r[0], r1 = r[1]; v = __builtin_bit_cast(float, r0) + __builtin_bit_cast(float, r1); }
    { const unsigned u = __builtin_bit_cast(unsigned, v); const unsigned u2 = u;
      const auto r = __builtin_amdgcn_permlane32_swap(u, u2, false, false);
      const unsigned r0 = r[0], r1 = r[1]; v = __builtin_bit_cast(float, r0) + __builtin_bit_cast(float, r1); }
    return v;
}

__device__ __forceinline__ unsigned lds_peek(unsigned addr) {
    unsigned v;
    asm volatile("ds_read_b32 %0, %1\n\ts_waitcnt lgkmcnt(0)" : "=&v"(v) : "v"(addr) : "memory");
    return __builtin_amdgcn_readfirstlane(v);
}
__device__ __forceinline__ void lds_inc(unsigned addr) {
    const unsigned one = 1;
    asm volatile("s_mov_b64 exec, 1\n\tds_add_u32 %0, %1\n\ts_mov_b64 exec, -1" :: "v"(addr), "v"(one) : "memory");
}
constexpr unsigned SPIN_CAP = 400000;
__device__ __forceinline__ bool spin_over(unsigned it, int lane, char* ws) {
    if ((it & 255u) != 255u) return false;
    GAS unsigned* err = (GAS unsigned*)(ws + WS_ERR);
    if (__builtin_amdgcn_readfirstlane(__hip_atomic_load(err, __ATOMIC_RELAXED, __HIP_MEMORY_SCOPE_AGENT)) == 0x600DBADu) return true;
    if (it > SPIN_CAP) { if (lane == 0) __hip_atomic_store(err, 0x600DBADu, __ATOMIC_RELAXED, __HIP_MEMORY_SCOPE_AGENT); return true; }
    return false;
}
#define W_AUX 0
#define A_AUX 16
#define ATAIL 2
#define CPRE 3
#define XSTEP 1
#define XPRE 3
#define XINC 2
#define ROT_W 1
#define ROT_W2 0
#define ROT_C 2
#define ROT_X 1

__device__ __forceinline__ void poll_n(const char* p  , int n, u64 tag, int lane, char* ws) {
    const GAS u64* f = (const GAS u64*)p;
    for (unsigned it = 0;; ++it) {
        u64 v = tag;
        if (lane < n) v = __hip_atomic_load(f, __ATOMIC_RELAXED, __HIP_MEMORY_SCOPE_AGENT);
        if (__all(v == tag) || spin_over(it, lane, ws)) break;
        __builtin_amdgcn_s_sleep(1);
    }
}

__device__ __forceinline__ void signal_last(LAS unsigned* cnt, unsigned last, char* flag, u64 tag, int lane) {
    asm volatile("s_waitcnt vmcnt(0)" ::: "memory");
    unsigned old = 0;
    if (lane == 0) old = __hip_atomic_fetch_add(cnt, 1u, __ATOMIC_RELAXED, __HIP_MEMORY_SCOPE_WORKGROUP);
    old = __builtin_amdgcn_readfirstlane(old);
    if (old == last && lane == 0) __hip_atomic_store((GAS u64*)flag, tag, __ATOMIC_RELAXED, __HIP_MEMORY_SCOPE_AGENT);
}

#define ST16_SC1(rs, v, voff, soff) __builtin_amdgcn_raw_buffer_store_b128((v), (rs), (voff) + (soff), 0, 16)
#define ST8_SC1(rs, v, voff, soff) __builtin_amdgcn_raw_buffer_store_b64((v), (rs), (voff) + (soff), 0, 16)
#define LD16x4_SC1(d0, d1, d2, d3, rs, voff) asm volatile("s_nop 4\n\tbuffer_load_dwordx4 %0, %4, %5, 0 offen sc1\n\tbuffer_load_dwordx4 %1, %4, %5, 0 offen offset:1024 sc1\n\t" \
    "buffer_load_dwordx4 %2, %4, %5, 0 offen offset:2048 sc1\n\tbuffer_load_dwordx4 %3, %4, %5, 0 offen offset:3072 sc1" \
    : "=&v"(d0), "=&v"(d1), "=&v"(d2), "=&v"(d3) : "v"(voff), "s"(rs) : "memory")
#define LD16x2_SC1(d0, d1, rs, voff) asm volatile("s_nop 4\n\tbuffer_load_dwordx4 %0, %2, %3, 0 offen sc1\n\tbuffer_load_dwordx4 %1, %2, %3, 0 offen offset:1024 sc1" \
    : "=&v"(d0), "=&v"(d1) : "v"(voff), "s"(rs) : "memory")
#define GLDS_ASM(rs, voff, soff, ldsaddr) do { unsigned keep_; \
    asm volatile("s_mov_b32 %0, m0\n\ts_mov_b32 m0, %2\n\ts_nop 4\n\tbuffer_load_dwordx4 %1, %3, %4 offen lds\n\ts_mov_b32 m0, %0" \
                 : "=&s"(keep_) : "v"(voff), "s"(ldsaddr), "s"(rs), "s"(soff) : "memory"); } while (0)
#define WAIT_VR(n, r) asm volatile("s_waitcnt vmcnt(%1)" : "+v"(r) : "n"(n) : "memory")

#define STAMP0(i) do { } while (0)
#define STAMP(i) do { } while (0)
#define STAMP2(i) do { } while (0)
#define STAMPC(i) do { } while (0)
#define STAMP_ARG
#define STAMP_PASS

__device__ __forceinline__ void load_c(LAS unsigned char* smem, const u16* C, int lw, int lane, int rot) {
#if defined(__HIP_DEVICE_COMPILE__)
    const __amdgpu_buffer_rsrc_t rC = __builtin_amdgcn_make_buffer_rsrc((void*)C, 0, 80 * DM * 2, 0x00020000);
    int vo[3];
    #pragma unroll
    for (int q = 0; q < 3; ++q) {
        const int rg = q < 2 ? lw + 4 * q : 8 + (lw & 1);
        const int row = 8 * rg + (lane >> 3);
        vo[q] = perm80(row) * (DM * 2) + (((lane & 7) ^ ((row >> 1) & 7)) * 16);
    }
    const unsigned sb = (unsigned)(size_t)smem + L_C;
    #pragma unroll
    for (int k0 = 0; k0 < 10; ++k0) {
        int ks = k0 + rot; ks = ks >= 10 ? ks - 10 : ks;
        GLDS_ASM(rC, vo[0], ks * 128, sb + ks * 10240 + lw * 1024);
        GLDS_ASM(rC, vo[1], ks * 128, sb + ks * 10240 + (lw + 4) * 1024);
        if ((k0 & 1) == (lw >> 1)) GLDS_ASM(rC, vo[2], ks * 128, sb + ks * 10240 + (8 + (lw & 1)) * 1024);
    }
#endif
}
__device__ __forceinline__ void publish_x(LAS unsigned char* smem, char* ws, u64 gen, int ln, int tbS, int e2, int kq, int w, int lane,
                                          const u32x4& xa, const u32x4& xb, const u32x2& xc) {
#if defined(__HIP_DEVICE_COMPILE__)
    const int wm = w & 1, wn = w >> 1, fr = lane & 15, fq = lane >> 4;
    const __amdgpu_buffer_rsrc_t rX = __builtin_amdgcn_make_buffer_rsrc((void*)(ws + WS_XN + (size_t)ln * XN_BYTES + (size_t)tbS * 163840 + (4 * wm + kq) * 20480), 0, 20480, 0x00020000);
    const int cc0 = 20 * e2 + 10 * wn;
    ST16_SC1(rX, xa, (cc0 + fq) * 256 + fr * 16, 0);
    ST16_SC1(rX, xb, (cc0 + 4 + fq) * 256 + fr * 16, 0);
    ST8_SC1(rX, xc, (cc0 + 8 + (fq >> 1)) * 256 + fr * 16 + (fq & 1) * 8, 0);
    float v[20];
    #pragma unroll
    for (int q = 0; q < 4; ++q) { v[2 * q] = bf_lo(xa[q]); v[2 * q + 1] = bf_hi(xa[q]); v[8 + 2 * q] = bf_lo(xb[q]); v[9 + 2 * q] = bf_hi(xb[q]); }
    v[16] = bf_lo(xc[0]); v[17] = bf_hi(xc[0]); v[18] = bf_lo(xc[1]); v[19] = bf_hi(xc[1]);
    float s = 0.f;
    #pragma unroll
    for (int q = 0; q < 20; ++q) s += v[q];
    const float mean = sum_mod16(s) * (1.0f / 80.0f);
    float m2 = 0.f;
    #pragma unroll
    for (int q = 0; q < 20; ++q) { const float d = v[q] - mean; m2 += d * d; }
    m2 = sum_mod16(m2);
    const __amdgpu_buffer_rsrc_t rS = __builtin_amdgcn_make_buffer_rsrc((void*)(ws + WS_ST + ((size_t)(ln * 16 + tbS) * 8 + 2 * e2 + wn) * 1024), 0, 1024, 0x00020000);
    const u32x2 st = {__builtin_bit_cast(unsigned, mean), __builtin_bit_cast(unsigned, m2)};
    ST8_SC1(rS, st, lane < 16 ? (64 * wm + 16 * kq + lane) * 8 : 0x7ffffff0, 0);
    signal_last((LAS unsigned*)(smem + L_MISC + CNT_PUB), (unsigned)ln * 4 + 3, ws + WS_FX + ((size_t)(ln * 16 + tbS) * 16 + e2 * 4 + kq) * 128, gen + 1 + ln, lane);
#endif
}

}

__global__ __launch_bounds__(512, 2) void fnet_persistent(const u16* __restrict__ x_in,
        const u16* __restrict__ WreA, const u16* __restrict__ WimA, const u16* __restrict__ CreA, const u16* __restrict__ CimA,
        const u16* __restrict__ WreB, const u16* __restrict__ WimB, const u16* __restrict__ CreB, const u16* __restrict__ CimB,
        u16* __restrict__ out, char* __restrict__ ws) {
#if defined(__HIP_DEVICE_COMPILE__)
    extern __shared__ __attribute__((aligned(16))) unsigned char smem_[];
    LAS unsigned char* smem = (LAS unsigned char*)smem_;
    const int tid = threadIdx.x;
    int lane = tid & 63;
    const int w0 = __builtin_amdgcn_readfirstlane(tid >> 6);
    const int b = blockIdx.x;
#define GEOM(bb) const int xcd = (bb) & 7, idx = (bb) >> 3; \
    const int kq = xcd & 3, tbS = (xcd >> 2) * 8 + (idx >> 2), e2 = idx & 3; \
    const int tbH = (xcd >> 1) * 4 + (idx >> 3), j = (xcd & 1) * 8 + (idx & 7); \
    const int halfS = kq >> 1, halfH = j >> 3, eb = j & 7; (void)halfS; (void)halfH; (void)eb; (void)e2; (void)tbS;

    STAMP0(0);
    u32x4 xa = {0, 0, 0, 0}, xb = {0, 0, 0, 0}; u32x2 xc = {0, 0};
    u64 gen;
    {
        GEOM(b)
        const int w = w0;
        if (w == 7) {
            u64 pc; asm volatile("s_getpc_b64 %0" : "=s"(pc));
            const __amdgpu_buffer_rsrc_t r = __builtin_amdgcn_make_buffer_rsrc((void*)(pc & ~(u64)255), 0, CODEWARM, 0x00020000);
            const unsigned dst = (unsigned)(size_t)smem + 159744u;
            #pragma unroll
            for (int q = 0; q < (CODEWARM + 8191) / 8192; ++q) { unsigned keep_;
                asm volatile("s_mov_b32 %0, m0\n\ts_mov_b32 m0, %2\n\ts_nop 4\n\tbuffer_load_dword %1, %3, %4 offen lds\n\ts_mov_b32 m0, %0" : "=&s"(keep_) : "v"(lane * 128), "s"(dst), "s"(r), "s"(q * 8192) : "memory"); }
        }
        if (w < 4) {
            const u16* xp = x_in + (size_t)(tbS * 128 + 64 * (w & 1) + 16 * kq + (lane & 15)) * DM + 160 * e2 + 80 * (w >> 1);
            xa = *(const u32x4*)(xp + 8 * (lane >> 4)); xb = *(const u32x4*)(xp + 32 + 8 * (lane >> 4)); xc = *(const u32x2*)(xp + 64 + 4 * (lane >> 4));
        }
        u64 g0 = __hip_atomic_load((const GAS u64*)(ws + WS_GEN), __ATOMIC_RELAXED, __HIP_MEMORY_SCOPE_AGENT);
        if (tid < 256) *(LAS unsigned*)(smem + L_MISC + tid * 4) = 0;
        WAIT_L(0);
        BAR();
        if (w >= 4) load_c(smem, (halfH ? CimA : CreA) + (size_t)eb * 80 * DM, w - 4, lane, ROT_C * (tbH & 3));
        gen = ((u64)(unsigned)__builtin_amdgcn_readfirstlane((unsigned)(g0 >> 32)) << 32) | (unsigned)__builtin_amdgcn_readfirstlane((unsigned)g0);
        if (w < 4) {
            STAMP(1);
            publish_x(smem, ws, gen, 0, tbS, e2, kq, w, lane, xa, xb, xc);
        }
    }
    STAMP(2);

    for (int l = 0; l < 4; ++l) {
        const int set = l & 1;
        asm volatile("" : "+v"(lane));
        int bb = b; asm volatile("" : "+s"(bb));
        int w = w0; asm volatile("" : "+s"(w));
        GEOM(bb)
        STAMP(8 + 12 * l + 0);
        STAMPC(48 + 2 * l);
        {
            const int g = w ^ 4;
            const int fr = lane & 15, fq = lane >> 4;
            int rdC[2];
            #pragma unroll
            for (int kk = 0; kk < 2; ++kk) rdC[kk] = L_C + fr * 128 + (((4 * kk + fq) ^ ((fr >> 1) & 7)) * 16);
            const unsigned cc = (unsigned)(size_t)smem + L_MISC + CNT_C, cx = (unsigned)(size_t)smem + L_MISC + CNT_X, ccs = (unsigned)(size_t)smem + L_MISC + CNT_CS;
            if (w >= 4) {
                WAIT_V(0);
                lds_inc(cc);
                for (unsigned i2 = 0; lds_peek(cc) < 4u * (unsigned)(l + 1) && i2 < 400000000u; ++i2) __builtin_amdgcn_s_sleep(1);
                if (l < 2) {
                    const bf16x8 ones = {0x3f80, 0x3f80, 0x3f80, 0x3f80, 0x3f80, 0x3f80, 0x3f80, 0x3f80};
                    f32x4 cs[5];
                    #pragma unroll
                    for (int i = 0; i < 5; ++i) cs[i] = (f32x4){0.f, 0.f, 0.f, 0.f};
                    #pragma unroll
                    for (int q = 0; q < 5; ++q) {
                        const int ks = 4 * q + (w & 3);
                        #pragma unroll
                        for (int i = 0; i < 5; ++i)
                            cs[i] = __builtin_amdgcn_mfma_f32_16x16x32_bf16(ones, *(const LAS bf16x8*)(smem + ((ks & 1) ? rdC[1] : rdC[0]) + (ks >> 1) * 10240 + i * 2048), cs[i], 0, 0, 0);
                    }
                    if (fq == 0) {
                        #pragma unroll
                        for (int i = 0; i < 5; ++i) __hip_atomic_fetch_add((LAS float*)(smem + L_MISC + CSUM + set * 320) + 16 * i + fr, cs[i][0], __ATOMIC_RELAXED, __HIP_MEMORY_SCOPE_WORKGROUP);
                    }
                    WAIT_L(0);
                    lds_inc(ccs);
                }
                __builtin_amdgcn_s_setprio(2);
            }
            poll_n(ws + WS_FX + ((size_t)(l * 16 + tbH) * 16 + (lane & 3) * 4 + (g & 3)) * 128, 4, gen + 1 + l, lane, ws);
            const __amdgpu_buffer_rsrc_t rX = __builtin_amdgcn_make_buffer_rsrc((void*)(ws + WS_XN + (size_t)l * XN_BYTES + (size_t)tbH * 163840 + g * 20480), 0, 20480, 0x00020000);
            u32x4 xf[20];
            const int vo = lane * 16;
            const int rot = ROT_X * (j & 7);
            if (w < 4) for (unsigned i2 = 0; lds_peek(cx) < 4u * (unsigned)(l + 1) && i2 < 400000000u; ++i2) { }
            auto ldx = [&](int q) __attribute__((always_inline)) { int k2 = q + rot; k2 = k2 >= 10 ? k2 - 10 : k2; LD16x2_SC1(xf[2 * q], xf[2 * q + 1], rX, vo + k2 * 2048); };
            u32x2 sp0, sp1;
            { const __amdgpu_buffer_rsrc_t rS = __builtin_amdgcn_make_buffer_rsrc((void*)(ws + WS_ST + (size_t)(l * 16 + tbH) * 8192), 0, 8192, 0x00020000);
              const int so = (lane >> 4) * 1024 + (16 * g + (lane & 15)) * 8;
              asm volatile("s_nop 4\n\tbuffer_load_dwordx2 %0, %2, %4, 0 offen sc1\n\tbuffer_load_dwordx2 %1, %3, %4, 0 offen sc1"
                           : "=&v"(sp0), "=&v"(sp1) : "v"(so), "v"(so + 4096), "s"(rS) : "memory"); }
            #pragma unroll
            for (int q = 0; q < XPRE; ++q) ldx(q);
            STAMP2(1);
            f32x4 acc[5];
            #pragma unroll
            for (int i = 0; i < 5; ++i) acc[i] = (f32x4){0.f, 0.f, 0.f, 0.f};
            bf16x8 cf[CPRE + 1][5];
            auto rdc = [&](int ks) __attribute__((always_inline)) {
                int k2 = (ks >> 1) + rot; k2 = k2 >= 10 ? k2 - 10 : k2;
                #pragma unroll
                for (int i = 0; i < 5; ++i) cf[ks % (CPRE + 1)][i] = *(const LAS bf16x8*)(smem + rdC[ks & 1] + k2 * 10240 + i * 2048);
            };
            #pragma unroll
            for (int ks = 0; ks < CPRE; ++ks) rdc(ks);
            float mean = 0.f, rstd = 0.f, mr[4], rr[4];
            const float sg = halfH ? -1.0f : 1.0f;
            #pragma unroll
            for (int ks = 0; ks < 20; ++ks) {
                if (ks % XSTEP == 0 && ks / XSTEP + XPRE < 10) ldx(ks / XSTEP + XPRE);
                if (ks == XINC && w >= 4) lds_inc(cx);
                if (ks + CPRE < 20) rdc(ks + CPRE);
                WAIT_VR((2 * (ks / XSTEP + 1 + XPRE) < 20 ? 2 * (ks / XSTEP + 1 + XPRE) : 20) - ks - 1, xf[ks]);
                if (ks == 0) asm volatile("" : "+v"(sp0), "+v"(sp1));
                if (ks == 0) STAMP2(2);
                if (ks == 10) STAMP2(3);
                if (ks == 19) STAMP2(4);
                #pragma unroll
                for (int i = 0; i < 5; ++i) acc[i] = __builtin_amdgcn_mfma_f32_16x16x32_bf16(__builtin_bit_cast(bf16x8, xf[ks]), cf[ks % (CPRE + 1)][i], acc[i], 0, 0, 0);
                if (ks == 3) {
                    const unsigned a0 = sp0[0], a1 = sp0[1], b0 = sp1[0], b1 = sp1[1];
                    const float m0 = __builtin_bit_cast(float, a0), q0 = __builtin_bit_cast(float, a1), m1 = __builtin_bit_cast(float, b0), q1 = __builtin_bit_cast(float, b1);
                    mean = sum_mod16(m0 + m1) * 0.125f;
                    const float d0 = m0 - mean, d1 = m1 - mean;
                    rstd = 1.0f / sqrtf(sum_mod16((q0 + q1) + 80.0f * (d0 * d0 + d1 * d1)) * (1.0f / DM) + 1e-5f);
                    #pragma unroll
                    for (int r = 0; r < 4; ++r) {
                        mr[r] = __builtin_bit_cast(float, __builtin_amdgcn_ds_bpermute((4 * fq + r) * 4, __builtin_bit_cast(int, mean)));
                        rr[r] = sg * __builtin_bit_cast(float, __builtin_amdgcn_ds_bpermute((4 * fq + r) * 4, __builtin_bit_cast(int, rstd)));
                    }
                }
            }
            for (unsigned i2 = 0; lds_peek(ccs) < 4u * (unsigned)(l < 1 ? 1 : 2) && i2 < 400000000u; ++i2) { }
            STAMP2(5);
            unsigned d[5][2];
            #pragma unroll
            for (int i = 0; i < 5; ++i) {
                const float c = *((const LAS float*)(smem + L_MISC + CSUM + set * 320) + 16 * i + fr);
                d[i][0] = pk_bf16((acc[i][0] - mr[0] * c) * rr[0], (acc[i][1] - mr[1] * c) * rr[1]);
                d[i][1] = pk_bf16((acc[i][2] - mr[2] * c) * rr[2], (acc[i][3] - mr[3] * c) * rr[3]);
            }
            const int sigma = 2 * tbH + (g >> 2);
            const __amdgpu_buffer_rsrc_t rA = __builtin_amdgcn_make_buffer_rsrc((void*)(ws + WS_AB + (size_t)l * AB_BYTES + ((size_t)(halfH * 4 + (eb >> 1)) * 32 + sigma) * 20480 + (eb & 1) * 10240 + (g & 3) * 2560), 0, 2560, 0x00020000);
            #pragma unroll
            for (int p = 0; p < 2; ++p) {
                const auto r0 = __builtin_amdgcn_permlane16_swap(d[2 * p][0], d[2 * p + 1][0], false, false);
                const auto r1 = __builtin_amdgcn_permlane16_swap(d[2 * p][1], d[2 * p + 1][1], false, false);
                const u32x4 o = {r0[0], r1[0], r0[1], r1[1]};
                const int so = (16 * (fq & 1) + fr) * 32 + (fq >> 1) * 16;
                ST16_SC1(rA, o, so, p * 1024);
            }
            { const u32x2 o = {d[4][0], d[4][1]}; ST8_SC1(rA, o, fr * 32 + fq * 8, 2048); }
            STAMP2(6);
            if (w >= 4)
            signal_last((LAS unsigned*)(smem + L_MISC + (w >= 4 ? CNT_H0 : CNT_H1)), (unsigned)l * 4 + 3,
                        ws + WS_FH + (((size_t)(l * 16 + tbH) * 16 + j) * 2 + (g >> 2)) * 128, gen + 5 + l, lane);
            __builtin_amdgcn_s_setprio(0);
        }
        STAMP(8 + 12 * l + 1);
        STAMPC(49 + 2 * l);
        const int rotw = l < 2 ? ROT_W : ROT_W2;
        const char* hflag = ws + WS_FH + (((size_t)(l * 16 + 8 * (kq & 1) + ((lane & 15) >> 1)) * 16 + 8 * halfS + 2 * e2 + (lane & 1)) * 2) * 128;
        if (w >= 4) {
            const int lw = w - 4;
            const u16* W = set ? (halfS ? WimB : WreB) : (halfS ? WimA : WreA);
            const __amdgpu_buffer_rsrc_t rW = __builtin_amdgcn_make_buffer_rsrc((void*)(W + (size_t)tbS * 128 * SEQ + (kq & 1) * 1024), 0, 128 * SEQ * 2, 0x00020000);
            const __amdgpu_buffer_rsrc_t rA = __builtin_amdgcn_make_buffer_rsrc((void*)(ws + WS_AB + (size_t)l * AB_BYTES + ((size_t)(halfS * 4 + e2) * 32 + (kq & 1) * 16) * 20480), 0, 16 * 20480, 0x00020000);
            const int voffW0 = (lane >> 3) * (SEQ * 2) + (((lane & 7) ^ ((lane >> 4) & 7)) * 16);
            const int voffW1 = (lane >> 3) * (SEQ * 2) + (((lane & 7) ^ ((4 + (lane >> 4)) & 7)) * 16);
            const int voffW = (lw & 1) ? voffW1 : voffW0;
            const int voffA = lane * 16;
            auto sig = [&](int x) __attribute__((always_inline)) { return 2 * ((x + tbS + rotw * e2) & 7) + (x >> 3); };
            auto wrow = [&](int i) __attribute__((always_inline)) { return 64 * (i >> 3) + 16 * (((i >> 1) + kq) & 3) + 8 * (i & 1); };
            auto issue_w = [&](int x) __attribute__((always_inline)) {
                const int sg_ = sig(x), dst = w_slot(0) * ((x & 3) == 0) + w_slot(1) * ((x & 3) == 1) + w_slot(2) * ((x & 3) == 2) + w_slot(3) * ((x & 3) == 3);
                #pragma unroll
                for (int q = 0; q < 4; ++q) { const int i = lw + 4 * q;
                    __builtin_amdgcn_raw_ptr_buffer_load_lds(rW, (LAS void*)(smem + dst + i * 1024), 16, voffW, sg_ * 128 + wrow(i) * (SEQ * 2), 0, W_AUX); }
            };
            auto issue_a = [&](int x) __attribute__((always_inline)) {
                const int sg_ = sig(x), dst = a_slot(0) * ((x & 3) == 0) + a_slot(1) * ((x & 3) == 1) + a_slot(2) * ((x & 3) == 2) + a_slot(3) * ((x & 3) == 3);
                #pragma unroll
                for (int q = 0; q < 5; ++q) { const int i = lw + 4 * q;
                    __builtin_amdgcn_raw_ptr_buffer_load_lds(rA, (LAS void*)(smem + dst + i * 1024), 16, voffA, sg_ * 20480 + i * 1024, 0, A_AUX); }
            };
            const unsigned go = (unsigned)(size_t)smem + L_MISC + CNT_GO;
            if (lw == 0) {
                poll_n(ws + WS_FH + (((size_t)(l * 16 + 8 * (kq & 1) + ((tbS + rotw * e2) & 7)) * 16 + 8 * halfS + 2 * e2 + (lane & 1)) * 2) * 128, 2, gen + 5 + l, lane, ws);
                lds_inc(go);
            } else {
                #pragma unroll
                for (int q = 0; q < 11; ++q) {
                    const int n = (lw - 1) + 3 * q;
                    if (n < 32) { const int x = n >> 4, i = n & 15;
                        __builtin_amdgcn_raw_ptr_buffer_load_lds(rW, (LAS void*)(smem + (x ? w_slot(1) : w_slot(0)) + i * 1024), 16, (i & 1) ? voffW1 : voffW0, sig(x) * 128 + wrow(i) * (SEQ * 2), 0, W_AUX); }
                }
                for (unsigned i2 = 0; lds_peek(go) < (unsigned)(l + 1) && i2 < 40000000u; ++i2) __builtin_amdgcn_s_sleep(1);
            }
            issue_a(0);
            if (lw == 0) poll_n(hflag, 16, gen + 5 + l, lane, ws);
            WAIT_V(0);
            BAR();
            STAMP(8 + 12 * l + 3);
            issue_a(1); issue_w(2); issue_a(2); issue_w(3);
            WAIT_V(13); BAR(); issue_a(3);
            WAIT_V(9);  BAR(); issue_w(4); issue_a(4);
            for (int st = 2; st < 13; ++st) { WAIT_V(9); BAR(); issue_w(st + 3); issue_a(st + 3); }
            WAIT_V(9); BAR();
            __builtin_amdgcn_s_waitcnt(0x0F70); BAR();
            BAR();
            BAR();
            STAMP(8 + 12 * l + 4);
            if (l < 3) {
                const unsigned ce = (unsigned)(size_t)smem + L_MISC + CNT_E;
                for (unsigned i2 = 0; lds_peek(ce) < 4u * (unsigned)(l + 1) && i2 < 40000000u; ++i2) __builtin_amdgcn_s_sleep(1);
                load_c(smem, (set ? (halfH ? CimA : CreA) : (halfH ? CimB : CreB)) + (size_t)eb * 80 * DM, lw, lane, ROT_C * (tbH & 3));
            }
            STAMP2(14);
        } else {
            const int fr = lane & 15, fq = lane >> 4;
            const int wm = w & 1, wn = w >> 1;
            int rdT[2], rdE[2];
            #pragma unroll
            for (int kk = 0; kk < 2; ++kk) {
                rdT[kk] = (wm * 64 + fr) * 128 + (((kk * 4 + fq) ^ ((fr >> 1) & 7)) * 16);
                rdE[kk] = wn * 10240 + (2 * kk + (fq >> 1)) * 2560 + fr * 32 + (fq & 1) * 16;
            }
            f32x4 acc[5][4];
            #pragma unroll
            for (int i = 0; i < 5; ++i)
                #pragma unroll
                for (int jj = 0; jj < 4; ++jj) acc[i][jj] = (f32x4){0.f, 0.f, 0.f, 0.f};
            bf16x8 ft[2][4], fe[2][5];
            auto rd = [&](int kk, int slot) __attribute__((always_inline)) {
                #pragma unroll
                for (int jj = 0; jj < 4; ++jj) ft[kk][jj] = *(const LAS bf16x8*)(smem + w_slot(slot) + rdT[kk] + jj * 2048);
                #pragma unroll
                for (int i = 0; i < 5; ++i) fe[kk][i] = *(const LAS bf16x8*)(smem + a_slot(slot) + rdE[kk] + i * 512);
            };
            auto mm = [&](int kk) __attribute__((always_inline)) {
                #pragma unroll
                for (int i = 0; i < 5; ++i)
                    #pragma unroll
                    for (int jj = 0; jj < 4; ++jj) acc[i][jj] = __builtin_amdgcn_mfma_f32_16x16x32_bf16(fe[kk][i], ft[kk][jj], acc[i][jj], 0, 0, 0);
                #pragma unroll
                for (int q = 0; q < 9; ++q) { __builtin_amdgcn_sched_group_barrier(0x8, 2, 0); __builtin_amdgcn_sched_group_barrier(0x100, 1, 0); }
                __builtin_amdgcn_sched_group_barrier(0x8, 2, 0);
            };
            BAR();
            STAMP(8 + 12 * l + 3);
            STAMPC(40 + 2 * l);
            rd(0, 0);
            for (int s0 = 0; s0 < 16; s0 += 4) {
                #pragma unroll
                for (int u = 0; u < 4; ++u) {
                    rd(1, u);
                    mm(0);
                    SCHED();
                    if (u == 0 && s0 == 0)
                        signal_last((LAS unsigned*)(smem + L_MISC + CNT_H1), (unsigned)l * 4 + 3, ws + WS_FH + (((size_t)(l * 16 + tbH) * 16 + j) * 2 + 1) * 128, gen + 5 + l, lane);
                    if (u == 1 && s0 == 4 && w == 0) poll_n(hflag + 128, 16, gen + 5 + l, lane, ws);
                    BAR();
                    SCHED();
                    rd(0, (u + 1) & 3);
                    mm(1);
                    SCHED();
                }
            }
            WAIT_L(0);
            BAR();
            STAMP(8 + 12 * l + 4);
            STAMPC(41 + 2 * l);
            char* pb = ws + WS_P + (size_t)l * P_BYTES + (size_t)(tbS * 4 + e2) * (64 * 2560);
            const __amdgpu_buffer_rsrc_t rP = __builtin_amdgcn_make_buffer_rsrc((void*)pb, 0, 64 * 2560, 0x00020000);
            #pragma unroll
            for (int jj = 1; jj < 4; ++jj) {
                const int po = ((((kq + jj) & 3) * 4 + kq) * 4 + w) * 2560;
                const u32x4 o0 = {pk_bf16(acc[0][jj][0], acc[0][jj][1]), pk_bf16(acc[0][jj][2], acc[0][jj][3]), pk_bf16(acc[1][jj][0], acc[1][jj][1]), pk_bf16(acc[1][jj][2], acc[1][jj][3])};
                ST16_SC1(rP, o0, po + lane * 16, 0);
                const u32x4 o1 = {pk_bf16(acc[2][jj][0], acc[2][jj][1]), pk_bf16(acc[2][jj][2], acc[2][jj][3]), pk_bf16(acc[3][jj][0], acc[3][jj][1]), pk_bf16(acc[3][jj][2], acc[3][jj][3])};
                ST16_SC1(rP, o1, po + 1024 + lane * 16, 0);
                const u32x2 o2 = {pk_bf16(acc[4][jj][0], acc[4][jj][1]), pk_bf16(acc[4][jj][2], acc[4][jj][3])};
                ST8_SC1(rP, o2, po + 2048 + lane * 8, 0);
            }
            STAMP2(12);
            signal_last((LAS unsigned*)(smem + L_MISC + CNT_E), (unsigned)l * 4 + 3, ws + WS_FS + ((size_t)(l * 16 + tbS) * 16 + e2 * 4 + kq) * 128, gen + 9 + l, lane);
            STAMP(8 + 12 * l + 5);
            poll_n(ws + WS_FS + ((size_t)(l * 16 + tbS) * 16 + e2 * 4 + ((kq + 1 + lane) & 3)) * 128, 3, gen + 9 + l, lane, ws);
            STAMP(8 + 12 * l + 9);
            u32x4 pa[3], pbv[3]; u32x2 pc[3];
            #pragma unroll
            for (int dq = 1; dq < 4; ++dq) {
                const int po = ((kq * 4 + ((kq + dq) & 3)) * 4 + w) * 2560;
                asm volatile("s_nop 4\n\tbuffer_load_dwordx4 %0, %3, %5, 0 offen sc1\n\tbuffer_load_dwordx4 %1, %3, %5, 0 offen offset:1024 sc1\n\tbuffer_load_dwordx2 %2, %4, %5, 0 offen offset:2048 sc1"
                             : "=&v"(pa[dq - 1]), "=&v"(pbv[dq - 1]), "=&v"(pc[dq - 1]) : "v"(po + lane * 16), "v"(po + lane * 8), "s"(rP) : "memory");
            }
            asm volatile("s_waitcnt vmcnt(0)" : "+v"(pa[0]), "+v"(pa[1]), "+v"(pa[2]), "+v"(pbv[0]), "+v"(pbv[1]), "+v"(pbv[2]), "+v"(pc[0]), "+v"(pc[1]), "+v"(pc[2]) :: "memory");
            STAMP2(21);
            #pragma unroll
            for (int q = 0; q < 4; ++q) {
                const float a0 = acc[q >> 1][0][2 * (q & 1)], a1 = acc[q >> 1][0][2 * (q & 1) + 1];
                xa[q] = pk_bf16(bf_lo(xa[q]) + ((a0 + bf_lo(pa[0][q])) + (bf_lo(pa[1][q]) + bf_lo(pa[2][q]))), bf_hi(xa[q]) + ((a1 + bf_hi(pa[0][q])) + (bf_hi(pa[1][q]) + bf_hi(pa[2][q]))));
                const float c0 = acc[2 + (q >> 1)][0][2 * (q & 1)], c1 = acc[2 + (q >> 1)][0][2 * (q & 1) + 1];
                xb[q] = pk_bf16(bf_lo(xb[q]) + ((c0 + bf_lo(pbv[0][q])) + (bf_lo(pbv[1][q]) + bf_lo(pbv[2][q]))), bf_hi(xb[q]) + ((c1 + bf_hi(pbv[0][q])) + (bf_hi(pbv[1][q]) + bf_hi(pbv[2][q]))));
            }
            #pragma unroll
            for (int q = 0; q < 2; ++q) {
                const float a0 = acc[4][0][2 * q], a1 = acc[4][0][2 * q + 1];
                xc[q] = pk_bf16(bf_lo(xc[q]) + ((a0 + bf_lo(pc[0][q])) + (bf_lo(pc[1][q]) + bf_lo(pc[2][q]))), bf_hi(xc[q]) + ((a1 + bf_hi(pc[0][q])) + (bf_hi(pc[1][q]) + bf_hi(pc[2][q]))));
            }
            if (l < 3) publish_x(smem, ws, gen, l + 1, tbS, e2, kq, w, lane, xa, xb, xc);
            else {
                const __amdgpu_buffer_rsrc_t rO = __builtin_amdgcn_make_buffer_rsrc((void*)(out + (size_t)(tbS * 128 + 64 * wm + 16 * kq) * DM + 160 * e2 + 80 * wn), 0, 16 * DM * 2, 0x00020000);
                ST16_SC1(rO, xa, fr * (DM * 2) + 16 * fq, 0); ST16_SC1(rO, xb, fr * (DM * 2) + 64 + 16 * fq, 0); ST8_SC1(rO, xc, fr * (DM * 2) + 128 + 8 * fq, 0);
            }
        }
        STAMP(8 + 12 * l + 10);
    }
    WAIT_V(0);
    if (b == 0 && tid == 0) __hip_atomic_store((GAS u64*)(ws + WS_GEN), gen + 16, __ATOMIC_RELAXED, __HIP_MEMORY_SCOPE_AGENT);
#endif
}

extern "C" void kernel_launch(void* const* d_in, const int* in_sizes, int n_in,
                              void* d_out, int out_size, void* d_ws, size_t ws_size,
                              hipStream_t stream) {
    (void)in_sizes; (void)n_in; (void)out_size; (void)ws_size;
    (void)hipFuncSetAttribute((const void*)fnet_persistent, hipFuncAttributeMaxDynamicSharedMemorySize, LDS_BYTES);
    fnet_persistent<<<256, 512, LDS_BYTES, stream>>>(
        (const u16*)d_in[0],
        (const u16*)d_in[1], (const u16*)d_in[2], (const u16*)d_in[3], (const u16*)d_in[4],
        (const u16*)d_in[6], (const u16*)d_in[7], (const u16*)d_in[8], (const u16*)d_in[9],
        (u16*)d_out, (char*)d_ws);
}
```

```cpp
#include <hip/hip_runtime.h>
#include <hip/hip_bf16.h>
#include <stdint.h>

typedef unsigned short u16;
typedef unsigned long long u64;
typedef short bf16x8 __attribute__((ext_vector_type(8)));
typedef float f32x4 __attribute__((ext_vector_type(4)));
typedef float f32x2 __attribute__((ext_vector_type(2)));
typedef unsigned u32x4 __attribute__((ext_vector_type(4)));
typedef unsigned u32x2 __attribute__((ext_vector_type(2)));
typedef __bf16 bf16x2v __attribute__((ext_vector_type(2)));
#define LAS __attribute__((address_space(3)))
#define GAS __attribute__((address_space(1)))

namespace {

constexpr int SEQ = 2048, DM = 640;

constexpr size_t WS_GEN = 0;
constexpr size_t WS_ERR = 256;
constexpr size_t WS_FX = 4096;
constexpr size_t WS_FH = WS_FX + 4 * 256 * 128;
constexpr size_t WS_FS = WS_FH + 4 * 512 * 128;
constexpr size_t WS_ST = 1u << 20;
constexpr size_t XN_BYTES = (size_t)SEQ * DM * 2;
constexpr size_t WS_XN = 2u << 20;
constexpr size_t WS_AB = WS_XN + 4 * XN_BYTES;
constexpr size_t AB_BYTES = 2 * XN_BYTES;
constexpr size_t WS_P = WS_AB + 4 * AB_BYTES;
constexpr size_t P_BYTES = 4 * XN_BYTES;
constexpr size_t WS_END = WS_P + 4 * P_BYTES;

constexpr int L_MISC = 0;
constexpr int L_C = 4096;
__device__ __forceinline__ constexpr int w_slot(int s) { return s == 0 ? 126976 : s == 1 ? 143360 : s == 2 ? 65536 : 81920; }
__device__ __forceinline__ constexpr int a_slot(int s) { return s == 0 ? 106496 : s == 1 ? 4096 : s == 2 ? 24576 : 45056; }
constexpr int L_JUNK = 159744;
constexpr int LDS_BYTES = 163840;
constexpr int CNT_C = 0, CNT_H0 = 16, CNT_H1 = 32, CNT_GO = 48, CNT_PUB = 64, CNT_CS = 80, CNT_E = 96, CNT_X = 112, CSUM = 256;

#define WAIT_V(n) asm volatile("s_waitcnt vmcnt(%0)" ::"n"(n) : "memory")
#define WAIT_L(n) asm volatile("s_waitcnt lgkmcnt(%0)" ::"n"(n) : "memory")
#define BAR() do { asm volatile("" ::: "memory"); __builtin_amdgcn_s_barrier(); asm volatile("" ::: "memory"); } while (0)
#define SCHED() __builtin_amdgcn_sched_barrier(0)

__device__ __forceinline__ unsigned pk_bf16(float a, float b) {
    const f32x2 v = {a, b};
    return __builtin_bit_cast(unsigned, __builtin_convertvector(v, bf16x2v));
}
__device__ __forceinline__ float bf_lo(unsigned u) { return __builtin_bit_cast(float, u << 16); }
__device__ __forceinline__ float bf_hi(unsigned u) { return __builtin_bit_cast(float, u & 0xffff0000u); }

__device__ __forceinline__ int perm80(int r) {
    return r < 64 ? ((r >> 5) * 32 + ((r & 15) >> 2) * 8 + ((r >> 4) & 1) * 4 + (r & 3)) : r;
}

__device__ __forceinline__ float sum_mod16(float v) {
    { const unsigned u = __builtin_bit_cast(unsigned, v); const unsigned u2 = u;
      const auto r = __builtin_amdgcn_permlane16_swap(u, u2, false, false);
      const unsigned r0 = r[0], r1 = r[1]; v = __builtin_bit_cast(float, r0) + __builtin_bit_cast(float, r1); }
    { const unsigned u = __builtin_bit_cast(unsigned, v); const unsigned u2 = u;
      const auto r = __builtin_amdgcn_permlane32_swap(u, u2, false, false);
      const unsigned r0 = r[0], r1 = r[1]; v = __builtin_bit_cast(float, r0) + __builtin_bit_cast(float, r1); }
    return v;
}

__device__ __forceinline__ unsigned lds_peek(unsigned addr) {
    unsigned v;
    asm volatile("ds_read_b32 %0, %1\n\ts_waitcnt lgkmcnt(0)" : "=&v"(v) : "v"(addr) : "memory");
    return __builtin_amdgcn_readfirstlane(v);
}
__device__ __forceinline__ void lds_inc(unsigned addr) {
    const unsigned one = 1;
    asm volatile("s_mov_b64 exec, 1\n\tds_add_u32 %0, %1\n\ts_mov_b64 exec, -1" :: "v"(addr), "v"(one) : "memory");
}
constexpr unsigned SPIN_CAP = 1500000;
__device__ __forceinline__ bool spin_over(unsigned it, int lane, char* ws) {
    if ((it & 255u) != 255u) return false;
    GAS unsigned* err = (GAS unsigned*)(ws + WS_ERR);
    if (__builtin_amdgcn_readfirstlane(__hip_atomic_load(err, __ATOMIC_RELAXED, __HIP_MEMORY_SCOPE_AGENT)) == 0x600DBADu) return true;
    if (it > SPIN_CAP) { if (lane == 0) __hip_atomic_store(err, 0x600DBADu, __ATOMIC_RELAXED, __HIP_MEMORY_SCOPE_AGENT); return true; }
    return false;
}
constexpr int ROT_W = 2, ROT_W2 = 0;
constexpr int ROT_C = 2, ROT_X = 1;
constexpr int XPRE = 3, XINC = 4;
constexpr int CPRE = 3;
constexpr int CODE_BYTES = 20480;

__device__ __forceinline__ void poll_n(const char* p  , int n, u64 tag, int lane, char* ws) {
    const GAS u64* f = (const GAS u64*)p;
    for (unsigned it = 0;; ++it) {
        u64 v = tag;
        if (lane < n) v = __hip_atomic_load(f, __ATOMIC_RELAXED, __HIP_MEMORY_SCOPE_AGENT);
        if (__all(v == tag) || spin_over(it, lane, ws)) break;
        __builtin_amdgcn_s_sleep(1);
    }
}

__device__ __forceinline__ void signal_last(LAS unsigned* cnt, unsigned last, char* flag, u64 tag, int lane) {
    asm volatile("s_waitcnt vmcnt(0)" ::: "memory");
    unsigned old = 0;
    if (lane == 0) old = __hip_atomic_fetch_add(cnt, 1u, __ATOMIC_RELAXED, __HIP_MEMORY_SCOPE_WORKGROUP);
    old = __builtin_amdgcn_readfirstlane(old);
    if (old == last && lane == 0) __hip_atomic_store((GAS u64*)flag, tag, __ATOMIC_RELAXED, __HIP_MEMORY_SCOPE_AGENT);
}

#define ST16_SC1(rs, v, voff, soff) __builtin_amdgcn_raw_buffer_store_b128((v), (rs), (voff) + (soff), 0, 16)
#define ST8_SC1(rs, v, voff, soff) __builtin_amdgcn_raw_buffer_store_b64((v), (rs), (voff) + (soff), 0, 16)
#define LD16x2_SC1(d0, d1, rs, voff) asm volatile("s_nop 4\n\tbuffer_load_dwordx4 %0, %2, %3, 0 offen sc1\n\tbuffer_load_dwordx4 %1, %2, %3, 0 offen offset:1024 sc1" \
    : "=&v"(d0), "=&v"(d1) : "v"(voff), "s"(rs) : "memory")
#define GLDS_ASM(rs, voff, soff, ldsaddr) do { unsigned keep_; \
    asm volatile("s_mov_b32 %0, m0\n\ts_mov_b32 m0, %2\n\ts_nop 4\n\tbuffer_load_dwordx4 %1, %3, %4 offen lds\n\ts_mov_b32 m0, %0" \
                 : "=&s"(keep_) : "v"(voff), "s"(ldsaddr), "s"(rs), "s"(soff) : "memory"); } while (0)
#define WAIT_VR(n, r) asm volatile("s_waitcnt vmcnt(%1)" : "+v"(r) : "n"(n) : "memory")

__device__ __forceinline__ void load_c(LAS unsigned char* smem, const u16* C, int lw, int lane, int rot) {
#if defined(__HIP_DEVICE_COMPILE__)
    const __amdgpu_buffer_rsrc_t rC = __builtin_amdgcn_make_buffer_rsrc((void*)C, 0, 80 * DM * 2, 0x00020000);
    int vo[3];
    #pragma unroll
    for (int q = 0; q < 3; ++q) {
        const int rg = q < 2 ? lw + 4 * q : 8 + (lw & 1);
        const int row = 8 * rg + (lane >> 3);
        vo[q] = perm80(row) * (DM * 2) + (((lane & 7) ^ ((row >> 1) & 7)) * 16);
    }
    const unsigned sb = (unsigned)(size_t)smem + L_C;
    #pragma unroll
    for (int k0 = 0; k0 < 10; ++k0) {
        int ks = k0 + rot; ks = ks >= 10 ? ks - 10 : ks;
        GLDS_ASM(rC, vo[0], ks * 128, sb + ks * 10240 + lw * 1024);
        GLDS_ASM(rC, vo[1], ks * 128, sb + ks * 10240 + (lw + 4) * 1024);
        if ((k0 & 1) == (lw >> 1)) GLDS_ASM(rC, vo[2], ks * 128, sb + ks * 10240 + (8 + (lw & 1)) * 1024);
    }
#endif
}
__device__ __forceinline__ void publish_x(LAS unsigned char* smem, char* ws, u64 gen, int ln, int tbS, int e2, int kq, int w, int lane,
                                          const u32x4& xa, const u32x4& xb, const u32x2& xc) {
#if defined(__HIP_DEVICE_COMPILE__)
    const int wm = w & 1, wn = w >> 1, fr = lane & 15, fq = lane >> 4;
    const __amdgpu_buffer_rsrc_t rX = __builtin_amdgcn_make_buffer_rsrc((void*)(ws + WS_XN + (size_t)ln * XN_BYTES + (size_t)tbS * 163840 + (4 * wm + kq) * 20480), 0, 20480, 0x00020000);
    const int cc0 = 20 * e2 + 10 * wn;
    ST16_SC1(rX, xa, (cc0 + fq) * 256 + fr * 16, 0);
    ST16_SC1(rX, xb, (cc0 + 4 + fq) * 256 + fr * 16, 0);
    ST8_SC1(rX, xc, (cc0 + 8 + (fq >> 1)) * 256 + fr * 16 + (fq & 1) * 8, 0);
    float v[20];
    #pragma unroll
    for (int q = 0; q < 4; ++q) { v[2 * q] = bf_lo(xa[q]); v[2 * q + 1] = bf_hi(xa[q]); v[8 + 2 * q] = bf_lo(xb[q]); v[9 + 2 * q] = bf_hi(xb[q]); }
    v[16] = bf_lo(xc[0]); v[17] = bf_hi(xc[0]); v[18] = bf_lo(xc[1]); v[19] = bf_hi(xc[1]);
    float s = 0.f;
    #pragma unroll
    for (int q = 0; q < 20; ++q) s += v[q];
    const float mean = sum_mod16(s) * (1.0f / 80.0f);
    float m2 = 0.f;
    #pragma unroll
    for (int q = 0; q < 20; ++q) { const float d = v[q] - mean; m2 += d * d; }
    m2 = sum_mod16(m2);
    const __amdgpu_buffer_rsrc_t rS = __builtin_amdgcn_make_buffer_rsrc((void*)(ws + WS_ST + ((size_t)(ln * 16 + tbS) * 8 + 2 * e2 + wn) * 1024), 0, 1024, 0x00020000);
    const u32x2 st = {__builtin_bit_cast(unsigned, mean), __builtin_bit_cast(unsigned, m2)};
    ST8_SC1(rS, st, lane < 16 ? (64 * wm + 16 * kq + lane) * 8 : 0x7ffffff0, 0);
    signal_last((LAS unsigned*)(smem + L_MISC + CNT_PUB), (unsigned)ln * 4 + 3, ws + WS_FX + ((size_t)(ln * 16 + tbS) * 16 + e2 * 4 + kq) * 128, gen + 1 + ln, lane);
#endif
}

}

__global__ __launch_bounds__(512, 2) void fnet_persistent(const u16* __restrict__ x_in,
        const u16* __restrict__ WreA, const u16* __restrict__ WimA, const u16* __restrict__ CreA, const u16* __restrict__ CimA,
        const u16* __restrict__ WreB, const u16* __restrict__ WimB, const u16* __restrict__ CreB, const u16* __restrict__ CimB,
        u16* __restrict__ out, char* __restrict__ ws) {
#if defined(__HIP_DEVICE_COMPILE__)
    extern __shared__ __attribute__((aligned(16))) unsigned char smem_[];
    LAS unsigned char* smem = (LAS unsigned char*)smem_;
    const int tid = threadIdx.x;
    int lane = tid & 63;
    const int w0 = __builtin_amdgcn_readfirstlane(tid >> 6);
    const int b = blockIdx.x;
#define GEOM(bb) const int xcd = (bb) & 7, idx = (bb) >> 3; \
    const int kq = xcd & 3, tbS = (xcd >> 2) * 8 + (idx >> 2), e2 = idx & 3; \
    const int tbH = (xcd >> 1) * 4 + (idx >> 3), j = (xcd & 1) * 8 + (idx & 7); \
    const int halfS = kq >> 1, halfH = j >> 3, eb = j & 7; (void)halfS; (void)halfH; (void)eb; (void)e2; (void)tbS;
    u32x4 xa = {0, 0, 0, 0}, xb = {0, 0, 0, 0}; u32x2 xc = {0, 0};
    u64 gen;
    {
        GEOM(b)
        const int w = w0;
        if (w == 7) {
            u64 pc; asm volatile("s_getpc_b64 %0" : "=s"(pc));
            const __amdgpu_buffer_rsrc_t r = __builtin_amdgcn_make_buffer_rsrc((void*)(pc & ~(u64)255), 0, CODE_BYTES, 0x00020000);
            const unsigned dst = (unsigned)(size_t)smem + L_JUNK;
            #pragma unroll
            for (int q = 0; q < (CODE_BYTES + 8191) / 8192; ++q) { unsigned keep_;
                asm volatile("s_mov_b32 %0, m0\n\ts_mov_b32 m0, %2\n\ts_nop 4\n\tbuffer_load_dword %1, %3, %4 offen lds\n\ts_mov_b32 m0, %0" : "=&s"(keep_) : "v"(lane * 128), "s"(dst), "s"(r), "s"(q * 8192) : "memory"); }
        }
        if (w < 4) {
            const u16* xp = x_in + (size_t)(tbS * 128 + 64 * (w & 1) + 16 * kq + (lane & 15)) * DM + 160 * e2 + 80 * (w >> 1);
            xa = *(const u32x4*)(xp + 8 * (lane >> 4)); xb = *(const u32x4*)(xp + 32 + 8 * (lane >> 4)); xc = *(const u32x2*)(xp + 64 + 4 * (lane >> 4));
        }
        u64 g0 = __hip_atomic_load((const GAS u64*)(ws + WS_GEN), __ATOMIC_RELAXED, __HIP_MEMORY_SCOPE_AGENT);
        if (tid < 64) *(LAS unsigned*)(smem + L_MISC + tid * 4) = 0;
        WAIT_L(0);
        BAR();
        if (w >= 4) load_c(smem, (halfH ? CimA : CreA) + (size_t)eb * 80 * DM, w - 4, lane, ROT_C * (tbH & 3));
        gen = ((u64)(unsigned)__builtin_amdgcn_readfirstlane((unsigned)(g0 >> 32)) << 32) | (unsigned)__builtin_amdgcn_readfirstlane((unsigned)g0);
        if (w < 4) publish_x(smem, ws, gen, 0, tbS, e2, kq, w, lane, xa, xb, xc);
    }
    for (int l = 0; l < 4; ++l) {
        const int set = l & 1;
        asm volatile("" : "+v"(lane));
        int bb = b; asm volatile("" : "+s"(bb));
        int w = w0; asm volatile("" : "+s"(w));
        GEOM(bb)
        {
            const int g = w ^ 4;
            const int fr = lane & 15, fq = lane >> 4;
            int rdC[2];
            #pragma unroll
            for (int kk = 0; kk < 2; ++kk) rdC[kk] = L_C + fr * 128 + (((4 * kk + fq) ^ ((fr >> 1) & 7)) * 16);
            const unsigned cc = (unsigned)(size_t)smem + L_MISC + CNT_C, cx = (unsigned)(size_t)smem + L_MISC + CNT_X, ccs = (unsigned)(size_t)smem + L_MISC + CNT_CS;
            if (w >= 4) {
                WAIT_V(0);
                lds_inc(cc);
                for (unsigned i2 = 0; lds_peek(cc) < 4u * (unsigned)(l + 1) && i2 < 400000000u; ++i2) __builtin_amdgcn_s_sleep(1);
                if (l < 2) {
                    const bf16x8 ones = {0x3f80, 0x3f80, 0x3f80, 0x3f80, 0x3f80, 0x3f80, 0x3f80, 0x3f80};
                    f32x4 cs[5];
                    #pragma unroll
                    for (int i = 0; i < 5; ++i) cs[i] = (f32x4){0.f, 0.f, 0.f, 0.f};
                    #pragma unroll
                    for (int q = 0; q < 5; ++q) {
                        const int ks = 4 * q + (w & 3);
                        #pragma unroll
                        for (int i = 0; i < 5; ++i)
                            cs[i] = __builtin_amdgcn_mfma_f32_16x16x32_bf16(ones, *(const LAS bf16x8*)(smem + ((ks & 1) ? rdC[1] : rdC[0]) + (ks >> 1) * 10240 + i * 2048), cs[i], 0, 0, 0);
                    }
                    if (fq == 0) {
                        #pragma unroll
                        for (int i = 0; i < 5; ++i) *((LAS float*)(smem + L_MISC + CSUM + set * 1280 + (w & 3) * 320) + 16 * i + fr) = cs[i][0];
                    }
                    WAIT_L(0);
                    lds_inc(ccs);
                    for (unsigned i2 = 0; lds_peek(ccs) < 4u * (unsigned)(l + 1) && i2 < 400000000u; ++i2) __builtin_amdgcn_s_sleep(1);
                }
                __builtin_amdgcn_s_setprio(2);
            }
            poll_n(ws + WS_FX + ((size_t)(l * 16 + tbH) * 16 + (lane & 3) * 4 + (g & 3)) * 128, 4, gen + 1 + l, lane, ws);
            const __amdgpu_buffer_rsrc_t rX = __builtin_amdgcn_make_buffer_rsrc((void*)(ws + WS_XN + (size_t)l * XN_BYTES + (size_t)tbH * 163840 + g * 20480), 0, 20480, 0x00020000);
            u32x4 xf[20];
            const int vo = lane * 16;
            const int rot = ROT_X * (j & 7);
            if (w < 4) for (unsigned i2 = 0; lds_peek(cx) < 4u * (unsigned)(l + 1) && i2 < 400000000u; ++i2) { }
            auto ldx = [&](int q) __attribute__((always_inline)) { int k2 = q + rot; k2 = k2 >= 10 ? k2 - 10 : k2; LD16x2_SC1(xf[2 * q], xf[2 * q + 1], rX, vo + k2 * 2048); };
            u32x2 sp0, sp1;
            { const __amdgpu_buffer_rsrc_t rS = __builtin_amdgcn_make_buffer_rsrc((void*)(ws + WS_ST + (size_t)(l * 16 + tbH) * 8192), 0, 8192, 0x00020000);
              const int so = (lane >> 4) * 1024 + (16 * g + (lane & 15)) * 8;
              asm volatile("s_nop 4\n\tbuffer_load_dwordx2 %0, %2, %4, 0 offen sc1\n\tbuffer_load_dwordx2 %1, %3, %4, 0 offen sc1"
                           : "=&v"(sp0), "=&v"(sp1) : "v"(so), "v"(so + 4096), "s"(rS) : "memory"); }
            #pragma unroll
            for (int q = 0; q < XPRE; ++q) ldx(q);
            f32x4 acc[5];
            #pragma unroll
            for (int i = 0; i < 5; ++i) acc[i] = (f32x4){0.f, 0.f, 0.f, 0.f};
            bf16x8 cf[CPRE + 1][5];
            auto rdc = [&](int ks) __attribute__((always_inline)) {
                int k2 = (ks >> 1) + rot; k2 = k2 >= 10 ? k2 - 10 : k2;
                #pragma unroll
                for (int i = 0; i < 5; ++i) cf[ks % (CPRE + 1)][i] = *(const LAS bf16x8*)(smem + rdC[ks & 1] + k2 * 10240 + i * 2048);
            };
            #pragma unroll
            for (int ks = 0; ks < CPRE; ++ks) rdc(ks);
            float csum[5];
            #pragma unroll
            for (int i = 0; i < 5; ++i) { const LAS float* cp = (const LAS float*)(smem + L_MISC + CSUM + set * 1280) + 16 * i + fr; csum[i] = (cp[0] + cp[80]) + (cp[160] + cp[240]); }
            float mean = 0.f, rstd = 0.f, mr[4], rr[4];
            const float sg = halfH ? -1.0f : 1.0f;
            #pragma unroll
            for (int ks = 0; ks < 20; ++ks) {
                if (ks + XPRE < 10) ldx(ks + XPRE);
                if (ks == XINC && w >= 4) lds_inc(cx);
                if (ks + CPRE < 20) rdc(ks + CPRE);
                WAIT_VR((ks + 1 + XPRE < 10 ? 2 * (ks + 1 + XPRE) : 20) - ks - 1, xf[ks]);
                if (ks == 0) asm volatile("" : "+v"(sp0), "+v"(sp1));
                #pragma unroll
                for (int i = 0; i < 5; ++i) acc[i] = __builtin_amdgcn_mfma_f32_16x16x32_bf16(__builtin_bit_cast(bf16x8, xf[ks]), cf[ks % (CPRE + 1)][i], acc[i], 0, 0, 0);
                if (ks == 3) {
                    const unsigned a0 = sp0[0], a1 = sp0[1], b0 = sp1[0], b1 = sp1[1];
                    const float m0 = __builtin_bit_cast(float, a0), q0 = __builtin_bit_cast(float, a1), m1 = __builtin_bit_cast(float, b0), q1 = __builtin_bit_cast(float, b1);
                    mean = sum_mod16(m0 + m1) * 0.125f;
                    const float d0 = m0 - mean, d1 = m1 - mean;
                    rstd = 1.0f / sqrtf(sum_mod16((q0 + q1) + 80.0f * (d0 * d0 + d1 * d1)) * (1.0f / DM) + 1e-5f);
                    #pragma unroll
                    for (int r = 0; r < 4; ++r) {
                        mr[r] = __builtin_bit_cast(float, __builtin_amdgcn_ds_bpermute((4 * fq + r) * 4, __builtin_bit_cast(int, mean)));
                        rr[r] = sg * __builtin_bit_cast(float, __builtin_amdgcn_ds_bpermute((4 * fq + r) * 4, __builtin_bit_cast(int, rstd)));
                    }
                }
            }
            unsigned d[5][2];
            #pragma unroll
            for (int i = 0; i < 5; ++i) {
                const float c = csum[i];
                d[i][0] = pk_bf16((acc[i][0] - mr[0] * c) * rr[0], (acc[i][1] - mr[1] * c) * rr[1]);
                d[i][1] = pk_bf16((acc[i][2] - mr[2] * c) * rr[2], (acc[i][3] - mr[3] * c) * rr[3]);
            }
            const int sigma = 2 * tbH + (g >> 2);
            const __amdgpu_buffer_rsrc_t rA = __builtin_amdgcn_make_buffer_rsrc((void*)(ws + WS_AB + (size_t)l * AB_BYTES + ((size_t)(halfH * 4 + (eb >> 1)) * 32 + sigma) * 20480 + (eb & 1) * 10240 + (g & 3) * 2560), 0, 2560, 0x00020000);
            #pragma unroll
            for (int p = 0; p < 2; ++p) {
                const auto r0 = __builtin_amdgcn_permlane16_swap(d[2 * p][0], d[2 * p + 1][0], false, false);
                const auto r1 = __builtin_amdgcn_permlane16_swap(d[2 * p][1], d[2 * p + 1][1], false, false);
                const u32x4 o = {r0[0], r1[0], r0[1], r1[1]};
                const int so = (16 * (fq & 1) + fr) * 32 + (fq >> 1) * 16;
                ST16_SC1(rA, o, so, p * 1024);
            }
            { const u32x2 o = {d[4][0], d[4][1]}; ST8_SC1(rA, o, fr * 32 + fq * 8, 2048); }
            if (w >= 4) signal_last((LAS unsigned*)(smem + L_MISC + CNT_H0), (unsigned)l * 4 + 3, ws + WS_FH + (((size_t)(l * 16 + tbH) * 16 + j) * 2) * 128, gen + 5 + l, lane);
            __builtin_amdgcn_s_setprio(0);
        }
        const int rotw = l < 2 ? ROT_W : ROT_W2;
        const char* hflag = ws + WS_FH + (((size_t)(l * 16 + 8 * (kq & 1) + ((lane & 15) >> 1)) * 16 + 8 * halfS + 2 * e2 + (lane & 1)) * 2) * 128;
        if (w >= 4) {
            const int lw = w - 4;
            const u16* W = set ? (halfS ? WimB : WreB) : (halfS ? WimA : WreA);
            const __amdgpu_buffer_rsrc_t rW = __builtin_amdgcn_make_buffer_rsrc((void*)(W + (size_t)tbS * 128 * SEQ + (kq & 1) * 1024), 0, 128 * SEQ * 2, 0x00020000);
            const __amdgpu_buffer_rsrc_t rA = __builtin_amdgcn_make_buffer_rsrc((void*)(ws + WS_AB + (size_t)l * AB_BYTES + ((size_t)(halfS * 4 + e2) * 32 + (kq & 1) * 16) * 20480), 0, 16 * 20480, 0x00020000);
            const int voffW0 = (lane >> 3) * (SEQ * 2) + (((lane & 7) ^ ((lane >> 4) & 7)) * 16);
            const int voffW1 = (lane >> 3) * (SEQ * 2) + (((lane & 7) ^ ((4 + (lane >> 4)) & 7)) * 16);
            const int voffW = (lw & 1) ? voffW1 : voffW0;
            const int voffA = lane * 16;
            auto sig = [&](int x) __attribute__((always_inline)) { return 2 * ((x + tbS + rotw * e2) & 7) + (x >> 3); };
            auto wrow = [&](int i) __attribute__((always_inline)) { return 64 * (i >> 3) + 16 * (((i >> 1) + kq) & 3) + 8 * (i & 1); };
            auto issue_w = [&](int x) __attribute__((always_inline)) {
                const int sg_ = sig(x), dst = w_slot(0) * ((x & 3) == 0) + w_slot(1) * ((x & 3) == 1) + w_slot(2) * ((x & 3) == 2) + w_slot(3) * ((x & 3) == 3);
                #pragma unroll
                for (int q = 0; q < 4; ++q) { const int i = lw + 4 * q;
                    __builtin_amdgcn_raw_ptr_buffer_load_lds(rW, (LAS void*)(smem + dst + i * 1024), 16, voffW, sg_ * 128 + wrow(i) * (SEQ * 2), 0, 0); }
            };
            auto issue_a = [&](int x) __attribute__((always_inline)) {
                const int sg_ = sig(x), dst = a_slot(0) * ((x & 3) == 0) + a_slot(1) * ((x & 3) == 1) + a_slot(2) * ((x & 3) == 2) + a_slot(3) * ((x & 3) == 3);
                #pragma unroll
                for (int q = 0; q < 5; ++q) { const int i = lw + 4 * q;
                    __builtin_amdgcn_raw_ptr_buffer_load_lds(rA, (LAS void*)(smem + dst + i * 1024), 16, voffA, sg_ * 20480 + i * 1024, 0, 16); }
            };
            const unsigned go = (unsigned)(size_t)smem + L_MISC + CNT_GO;
            if (lw == 0) {
                poll_n(ws + WS_FH + (((size_t)(l * 16 + 8 * (kq & 1) + ((((lane >> 1) & 1) + tbS + rotw * e2) & 7)) * 16 + 8 * halfS + 2 * e2 + (lane & 1)) * 2) * 128, 4, gen + 5 + l, lane, ws);
                lds_inc(go);
            } else {
                #pragma unroll
                for (int q = 0; q < 11; ++q) {
                    const int n = (lw - 1) + 3 * q;
                    if (n < 32) { const int x = n >> 4, i = n & 15;
                        __builtin_amdgcn_raw_ptr_buffer_load_lds(rW, (LAS void*)(smem + (x ? w_slot(1) : w_slot(0)) + i * 1024), 16, (i & 1) ? voffW1 : voffW0, sig(x) * 128 + wrow(i) * (SEQ * 2), 0, 0); }
                }
                for (unsigned i2 = 0; lds_peek(go) < (unsigned)(l + 1) && i2 < 40000000u; ++i2) __builtin_amdgcn_s_sleep(1);
            }
            issue_a(0);
            u32x4 a1[5];
            { const int sg_ = sig(1);
              #pragma unroll
              for (int q = 0; q < 5; ++q)
                  asm volatile("s_nop 4\n\tbuffer_load_dwordx4 %0, %1, %2, %3 offen sc1" : "=&v"(a1[q]) : "v"(voffA), "s"(rA), "s"(sg_ * 20480 + (lw + 4 * q) * 1024) : "memory"); }
            if (lw == 0) poll_n(hflag, 16, gen + 5 + l, lane, ws);
            asm volatile("s_waitcnt vmcnt(0)" : "+v"(a1[0]), "+v"(a1[1]), "+v"(a1[2]), "+v"(a1[3]), "+v"(a1[4]) :: "memory");
            BAR();
            { const unsigned dst = (unsigned)(size_t)smem + a_slot(1) + lw * 1024 + lane * 16;
              #pragma unroll
              for (int q = 0; q < 5; ++q) asm volatile("ds_write_b128 %0, %1 offset:%2" :: "v"(dst), "v"(a1[q]), "n"(q * 4096) : "memory"); }
            issue_a(2); issue_w(2);
            WAIT_L(0); BAR(); issue_w(3); issue_a(3);
            WAIT_V(9);  BAR(); issue_w(4); issue_a(4);
            for (int st = 2; st < 13; ++st) { WAIT_V(9); BAR(); issue_w(st + 3); issue_a(st + 3); }
            WAIT_V(9); BAR();
            __builtin_amdgcn_s_waitcnt(0x0F70); BAR();
            BAR();
            BAR();
            if (l < 3) {
                const unsigned ce = (unsigned)(size_t)smem + L_MISC + CNT_E;
                for (unsigned i2 = 0; lds_peek(ce) < 4u * (unsigned)(l + 1) && i2 < 40000000u; ++i2) __builtin_amdgcn_s_sleep(1);
                load_c(smem, (set ? (halfH ? CimA : CreA) : (halfH ? CimB : CreB)) + (size_t)eb * 80 * DM, lw, lane, ROT_C * (tbH & 3));
            }
        } else {
            const int fr = lane & 15, fq = lane >> 4;
            const int wm = w & 1, wn = w >> 1;
            int rdT[2], rdE[2];
            #pragma unroll
            for (int kk = 0; kk < 2; ++kk) {
                rdT[kk] = (wm * 64 + fr) * 128 + (((kk * 4 + fq) ^ ((fr >> 1) & 7)) * 16);
                rdE[kk] = wn * 10240 + (2 * kk + (fq >> 1)) * 2560 + fr * 32 + (fq & 1) * 16;
            }
            f32x4 acc[5][4];
            #pragma unroll
            for (int i = 0; i < 5; ++i)
                #pragma unroll
                for (int jj = 0; jj < 4; ++jj) acc[i][jj] = (f32x4){0.f, 0.f, 0.f, 0.f};
            bf16x8 ft[2][4], fe[2][5];
            auto rd = [&](int kk, int slot) __attribute__((always_inline)) {
                #pragma unroll
                for (int jj = 0; jj < 4; ++jj) ft[kk][jj] = *(const LAS bf16x8*)(smem + w_slot(slot) + rdT[kk] + jj * 2048);
                #pragma unroll
                for (int i = 0; i < 5; ++i) fe[kk][i] = *(const LAS bf16x8*)(smem + a_slot(slot) + rdE[kk] + i * 512);
            };
            auto mm = [&](int kk) __attribute__((always_inline)) {
                #pragma unroll
                for (int i = 0; i < 5; ++i)
                    #pragma unroll
                    for (int jj = 0; jj < 4; ++jj) acc[i][jj] = __builtin_amdgcn_mfma_f32_16x16x32_bf16(fe[kk][i], ft[kk][jj], acc[i][jj], 0, 0, 0);
                #pragma unroll
                for (int q = 0; q < 9; ++q) { __builtin_amdgcn_sched_group_barrier(0x8, 2, 0); __builtin_amdgcn_sched_group_barrier(0x100, 1, 0); }
                __builtin_amdgcn_sched_group_barrier(0x8, 2, 0);
            };
            BAR();
            u64 pv = 0;
            rd(0, 0);
            for (int s0 = 0; s0 < 16; s0 += 4) {
                #pragma unroll
                for (int u = 0; u < 4; ++u) {
                    rd(1, u);
                    mm(0);
                    SCHED();
                    if (u == 1 && s0 == 0)
                        signal_last((LAS unsigned*)(smem + L_MISC + CNT_H1), (unsigned)l * 4 + 3, ws + WS_FH + (((size_t)(l * 16 + tbH) * 16 + j) * 2 + 1) * 128, gen + 5 + l, lane);
                    if (u == 3 && s0 == 0 && w == 0) { pv = gen + 5 + l; if (lane < 16) pv = __hip_atomic_load((const GAS u64*)(hflag + 128), __ATOMIC_RELAXED, __HIP_MEMORY_SCOPE_AGENT); }
                    if (u == 1 && s0 == 4 && w == 0) { if (!__all(pv == gen + 5 + l)) poll_n(hflag + 128, 16, gen + 5 + l, lane, ws); }
                    BAR();
                    SCHED();
                    if (u < 3 || s0 < 12) {
                        rd(0, (u + 1) & 3);
                        mm(1);
                        SCHED();
                    }
                }
            }
            char* pb = ws + WS_P + (size_t)l * P_BYTES + (size_t)(tbS * 4 + e2) * (64 * 2560);
            const __amdgpu_buffer_rsrc_t rP = __builtin_amdgcn_make_buffer_rsrc((void*)pb, 0, 64 * 2560, 0x00020000);
            #pragma unroll
            for (int jq = 1; jq < 5; ++jq) {
                const int jj = jq & 3;
                #pragma unroll
                for (int i = 0; i < 5; ++i) acc[i][jj] = __builtin_amdgcn_mfma_f32_16x16x32_bf16(fe[1][i], ft[1][jj], acc[i][jj], 0, 0, 0);
                if (jq > 1) {
                    const int js = jq - 1;
                    const int po = ((((kq + js) & 3) * 4 + kq) * 4 + w) * 2560;
                    const u32x4 o0 = {pk_bf16(acc[0][js][0], acc[0][js][1]), pk_bf16(acc[0][js][2], acc[0][js][3]), pk_bf16(acc[1][js][0], acc[1][js][1]), pk_bf16(acc[1][js][2], acc[1][js][3])};
                    ST16_SC1(rP, o0, po + lane * 16, 0);
                    const u32x4 o1 = {pk_bf16(acc[2][js][0], acc[2][js][1]), pk_bf16(acc[2][js][2], acc[2][js][3]), pk_bf16(acc[3][js][0], acc[3][js][1]), pk_bf16(acc[3][js][2], acc[3][js][3])};
                    ST16_SC1(rP, o1, po + 1024 + lane * 16, 0);
                    const u32x2 o2 = {pk_bf16(acc[4][js][0], acc[4][js][1]), pk_bf16(acc[4][js][2], acc[4][js][3])};
                    ST8_SC1(rP, o2, po + 2048 + lane * 8, 0);
                }
                SCHED();
            }
            WAIT_L(0);
            BAR();
            signal_last((LAS unsigned*)(smem + L_MISC + CNT_E), (unsigned)l * 4 + 3, ws + WS_FS + ((size_t)(l * 16 + tbS) * 16 + e2 * 4 + kq) * 128, gen + 9 + l, lane);
            poll_n(ws + WS_FS + ((size_t)(l * 16 + tbS) * 16 + e2 * 4 + ((kq + 1 + lane) & 3)) * 128, 3, gen + 9 + l, lane, ws);
            u32x4 pa[3], pbv[3]; u32x2 pc[3];
            #pragma unroll
            for (int dq = 1; dq < 4; ++dq) {
                const int po = ((kq * 4 + ((kq + dq) & 3)) * 4 + w) * 2560;
                asm volatile("s_nop 4\n\tbuffer_load_dwordx4 %0, %3, %5, 0 offen sc1\n\tbuffer_load_dwordx4 %1, %3, %5, 0 offen offset:1024 sc1\n\tbuffer_load_dwordx2 %2, %4, %5, 0 offen offset:2048 sc1"
                             : "=&v"(pa[dq - 1]), "=&v"(pbv[dq - 1]), "=&v"(pc[dq - 1]) : "v"(po + lane * 16), "v"(po + lane * 8), "s"(rP) : "memory");
            }
            asm volatile("s_waitcnt vmcnt(0)" : "+v"(pa[0]), "+v"(pa[1]), "+v"(pa[2]), "+v"(pbv[0]), "+v"(pbv[1]), "+v"(pbv[2]), "+v"(pc[0]), "+v"(pc[1]), "+v"(pc[2]) :: "memory");
            #pragma unroll
            for (int q = 0; q < 4; ++q) {
                const float a0 = acc[q >> 1][0][2 * (q & 1)], a1 = acc[q >> 1][0][2 * (q & 1) + 1];
                xa[q] = pk_bf16(bf_lo(xa[q]) + ((a0 + bf_lo(pa[0][q])) + (bf_lo(pa[1][q]) + bf_lo(pa[2][q]))), bf_hi(xa[q]) + ((a1 + bf_hi(pa[0][q])) + (bf_hi(pa[1][q]) + bf_hi(pa[2][q]))));
                const float c0 = acc[2 + (q >> 1)][0][2 * (q & 1)], c1 = acc[2 + (q >> 1)][0][2 * (q & 1) + 1];
                xb[q] = pk_bf16(bf_lo(xb[q]) + ((c0 + bf_lo(pbv[0][q])) + (bf_lo(pbv[1][q]) + bf_lo(pbv[2][q]))), bf_hi(xb[q]) + ((c1 + bf_hi(pbv[0][q])) + (bf_hi(pbv[1][q]) + bf_hi(pbv[2][q]))));
            }
            #pragma unroll
            for (int q = 0; q < 2; ++q) {
                const float a0 = acc[4][0][2 * q], a1 = acc[4][0][2 * q + 1];
                xc[q] = pk_bf16(bf_lo(xc[q]) + ((a0 + bf_lo(pc[0][q])) + (bf_lo(pc[1][q]) + bf_lo(pc[2][q]))), bf_hi(xc[q]) + ((a1 + bf_hi(pc[0][q])) + (bf_hi(pc[1][q]) + bf_hi(pc[2][q]))));
            }
            if (l < 3) publish_x(smem, ws, gen, l + 1, tbS, e2, kq, w, lane, xa, xb, xc);
            else {
                const __amdgpu_buffer_rsrc_t rO = __builtin_amdgcn_make_buffer_rsrc((void*)(out + (size_t)(tbS * 128 + 64 * wm + 16 * kq) * DM + 160 * e2 + 80 * wn), 0, 16 * DM * 2, 0x00020000);
                ST16_SC1(rO, xa, fr * (DM * 2) + 16 * fq, 0); ST16_SC1(rO, xb, fr * (DM * 2) + 64 + 16 * fq, 0); ST8_SC1(rO, xc, fr * (DM * 2) + 128 + 8 * fq, 0);
            }
        }
    }
    WAIT_V(0);
    if (b == 0 && tid == 0) __hip_atomic_store((GAS u64*)(ws + WS_GEN), gen + 16, __ATOMIC_RELAXED, __HIP_MEMORY_SCOPE_AGENT);
#endif
}

extern "C" void kernel_launch(void* const* d_in, const int* in_sizes, int n_in,
                              void* d_out, int out_size, void* d_ws, size_t ws_size,
                              hipStream_t stream) {
    (void)in_sizes; (void)n_in; (void)out_size; (void)ws_size;
    (void)hipFuncSetAttribute((const void*)fnet_persistent, hipFuncAttributeMaxDynamicSharedMemorySize, LDS_BYTES);
    fnet_persistent<<<256, 512, LDS_BYTES, stream>>>(
        (const u16*)d_in[0],
        (const u16*)d_in[1], (const u16*)d_in[2], (const u16*)d_in[3], (const u16*)d_in[4],
        (const u16*)d_in[6], (const u16*)d_in[7], (const u16*)d_in[8], (const u16*)d_in[9],
        (u16*)d_out, (char*)d_ws);
}
```
